```python
import torch

def _softmax(x, axis=-1):
    return torch.softmax(x, dim=axis)
def _gelu(x):
    return torch.nn.functional.gelu(x, approximate="tanh")
def _silu(x):
    return torch.nn.functional.silu(x)
def _sigmoid(x):
    return torch.sigmoid(x)
def _layernorm(x, axis=-1, eps=1e-5):
    m = x.mean(dim=axis, keepdim=True)
    v = x.var(dim=axis, keepdim=True, unbiased=False)
    return (x - m) / torch.sqrt(v + eps)
def _scatter_add(tgt, idx, src, axis=0):
    return tgt.clone().index_add_(axis, idx, src)
def _segment_sum(src, seg, n):
    out = torch.zeros((n,) + src.shape[1:], dtype=src.dtype)
    return out.index_add_(0, seg, src)
def _pad(x, pw):
    flat = tuple(v for pair in reversed(pw) for v in pair)
    return torch.nn.functional.pad(x, flat)

kernel_name = 'interleave_layers__linear_attn_block__glu_gated_bilinear__prompt'

def setup_inputs():

    _w = (lambda a: (lambda t: t.to(torch.bfloat16) if t.is_floating_point() else t)(torch.as_tensor(a)))
    def _A_setup():
        B, S, D = 1, 2048, 640
        return {
            "x": _w((lambda *s: torch.randn(*s, dtype=torch.bfloat16) * 0.05)(B, S, D)),
            "wqkv": _w((lambda *s: torch.randn(*s, dtype=torch.bfloat16) * 0.05)(D, 3 * D)),
            "wo": _w((lambda *s: torch.randn(*s, dtype=torch.bfloat16) * 0.05)(D, D)),
        }
    _A_in = _A_setup()

    def _B_setup():
        B, S, D, F = 1, 2048, 640, 1728
        return {
            "x": _w((lambda *s: torch.randn(*s, dtype=torch.bfloat16) * 0.05)(B, S, D)),
            "w_gate": _w((lambda *s: torch.randn(*s, dtype=torch.bfloat16) * 0.8)(D, F) * (D ** -0.5)),
            "w_up": _w((lambda *s: torch.randn(*s, dtype=torch.bfloat16) * 0.8)(D, F) * (D ** -0.5)),
            "w_down": _w((lambda *s: torch.randn(*s, dtype=torch.bfloat16) * 0.8)(F, D) * (F ** -0.5)),
        }
    _B_in = _B_setup()

    return {
        "A_x": _A_in["x"],
        "A_wqkv": _A_in["wqkv"],
        "A_wo": _A_in["wo"],
        "B_x": _B_in["x"],
        "B_w_gate": _B_in["w_gate"],
        "B_w_up": _B_in["w_up"],
        "B_w_down": _B_in["w_down"],
    }

def reference(A_x, A_wqkv, A_wo, B_x, B_w_gate, B_w_up, B_w_down):
    def _A_ref(x, wqkv, wo):
        B, S, D = x.shape
        H = 32; Dh = D // H
        qkv = (lambda x, *d: x.permute(*d))((x @ wqkv).reshape(B, S, 3, H, Dh), 2, 0, 3, 1, 4)
        q, k, v = qkv[0], qkv[1], qkv[2]
        phi = lambda t: torch.where(t > 0, t + 1, torch.exp(t)) + 1e-6
        pq, pk = phi(q), phi(k)
        kv = torch.einsum("bhsd,bhse->bhde", pk, v)
        z = torch.einsum("bhsd->bhd", pk)
        num = torch.einsum("bhqd,bhde->bhqe", pq, kv)
        den = torch.einsum("bhqd,bhd->bhq", pq, z)[..., None] + 1e-6
        o = num / den
        return (lambda x, *d: x.permute(*d))(o, 0, 2, 1, 3).reshape(B, S, D) @ wo

    def _B_ref(x, w_gate, w_up, w_down):
        g = torch.einsum("bsd,df->bsf", x, w_gate)
        u = torch.einsum("bsd,df->bsf", x, w_up)
        h = g * u
        return torch.einsum("bsf,fd->bsd", h, w_down)

    x = A_x
    x = x + _A_ref(_layernorm(x, axis=-1), A_wqkv, A_wo)
    x = x + _B_ref(_layernorm(x, axis=-1), B_w_gate, B_w_up, B_w_down)
    x = x + _A_ref(_layernorm(x, axis=-1), A_wqkv, A_wo)
    x = x + _B_ref(_layernorm(x, axis=-1), B_w_gate, B_w_up, B_w_down)
    return x
```

```cpp
#include <hip/hip_runtime.h>
#include <hip/hip_bf16.h>
typedef unsigned short u16;
__device__ __forceinline__ float b2f(u16 v) { return __uint_as_float(((unsigned)v) << 16); }
__device__ __forceinline__ u16 f2b(float f) { __hip_bfloat16 h = __float2bfloat16(f); return *(u16*)&h; }
constexpr int S = 2048, D = 640, F = 1728, H = 32, DH = 20;

__global__ void ln_k(const u16* x, u16* y) {
    int row = blockIdx.x; __shared__ float red[256];
    float v[3]; float s = 0;
    for (int i = 0; i < 3; ++i) { int c = threadIdx.x + 256 * i; v[i] = c < D ? b2f(x[row * D + c]) : 0.f; s += v[i]; }
    red[threadIdx.x] = s; __syncthreads();
    for (int o = 128; o; o >>= 1) { if (threadIdx.x < o) red[threadIdx.x] += red[threadIdx.x + o]; __syncthreads(); }
    float m = red[0] / D; __syncthreads();
    float q = 0; for (int i = 0; i < 3; ++i) { int c = threadIdx.x + 256 * i; if (c < D) q += (v[i] - m) * (v[i] - m); }
    red[threadIdx.x] = q; __syncthreads();
    for (int o = 128; o; o >>= 1) { if (threadIdx.x < o) red[threadIdx.x] += red[threadIdx.x + o]; __syncthreads(); }
    float r = 1.f / sqrtf(red[0] / D + 1e-5f);
    for (int i = 0; i < 3; ++i) { int c = threadIdx.x + 256 * i; if (c < D) y[row * D + c] = f2b((v[i] - m) * r); }
}
__global__ void gemm_k(const u16* A, const u16* B, const u16* res, u16* C, int M, int N, int K) {
    __shared__ float As[16][17], Bs[16][17];
    int tx = threadIdx.x & 15, ty = threadIdx.x >> 4; int r = blockIdx.y * 16 + ty, c = blockIdx.x * 16 + tx; float acc = 0;
    for (int k0 = 0; k0 < K; k0 += 16) {
        As[ty][tx] = b2f(A[r * K + k0 + tx]); Bs[ty][tx] = b2f(B[(k0 + ty) * N + c]); __syncthreads();
        for (int k = 0; k < 16; ++k) acc += As[ty][k] * Bs[k][tx]; __syncthreads();
    }
    if (res) acc += b2f(res[r * N + c]);
    C[r * N + c] = f2b(acc);
}
__device__ float phi(float t) { return (t > 0 ? t + 1.f : __expf(t)) + 1e-6f; }
__global__ void kv_k(const u16* qkv, float* kv, float* z) {
    int h = blockIdx.x; int t = threadIdx.x;
    if (t < 400) { int d = t / 20, e = t % 20; float a = 0; for (int s = 0; s < S; ++s) a += phi(b2f(qkv[s * 1920 + 640 + h * 20 + d])) * b2f(qkv[s * 1920 + 1280 + h * 20 + e]); kv[h * 400 + t] = a; }
    else if (t < 420) { int d = t - 400; float a = 0; for (int s = 0; s < S; ++s) a += phi(b2f(qkv[s * 1920 + 640 + h * 20 + d])); z[h * 20 + d] = a; }
}
__global__ void o_k(const u16* qkv, const float* kv, const float* z, u16* o) {
    int s = blockIdx.x;
    for (int c = threadIdx.x; c < D; c += 256) { int h = c / 20, e = c % 20; float num = 0, den = 0;
        for (int d = 0; d < 20; ++d) { float p = phi(b2f(qkv[s * 1920 + h * 20 + d])); num += p * kv[h * 400 + d * 20 + e]; den += p * z[h * 20 + d]; }
        o[s * D + c] = f2b(num / (den + 1e-6f)); }
}
__global__ void mul_k(const u16* g, const u16* u, u16* h, int n) { int i = blockIdx.x * 256 + threadIdx.x; if (i < n) h[i] = f2b(b2f(g[i]) * b2f(u[i])); }

extern "C" void kernel_launch(void* const* d_in, const int* in_sizes, int n_in, void* d_out, int out_size, void* d_ws, size_t ws_size, hipStream_t stream) {
    const u16* x0 = (const u16*)d_in[0]; const u16* wqkv = (const u16*)d_in[1]; const u16* wo = (const u16*)d_in[2];
    const u16* wg = (const u16*)d_in[4]; const u16* wu = (const u16*)d_in[5]; const u16* wd = (const u16*)d_in[6];
    char* ws = (char*)d_ws; size_t MB = 1 << 20;
    u16* xn = (u16*)(ws); u16* qkv = (u16*)(ws + 4 * MB); float* kv = (float*)(ws + 16 * MB); float* z = (float*)(ws + 17 * MB);
    u16* o = (u16*)(ws + 18 * MB); u16* x1 = (u16*)(ws + 22 * MB); u16* g = (u16*)(ws + 26 * MB); u16* u = (u16*)(ws + 36 * MB); u16* hh = (u16*)(ws + 46 * MB);
    u16* x2 = (u16*)(ws + 56 * MB);
    const u16* x = x0;
    for (int l = 0; l < 2; ++l) {
        ln_k<<<S, 256, 0, stream>>>(x, xn);
        gemm_k<<<dim3(1920 / 16, S / 16), 256, 0, stream>>>(xn, wqkv, nullptr, qkv, S, 1920, D);
        kv_k<<<H, 448, 0, stream>>>(qkv, kv, z);
        o_k<<<S, 256, 0, stream>>>(qkv, kv, z, o);
        gemm_k<<<dim3(D / 16, S / 16), 256, 0, stream>>>(o, wo, x, x1, S, D, D);
        ln_k<<<S, 256, 0, stream>>>(x1, xn);
        gemm_k<<<dim3(F / 16, S / 16), 256, 0, stream>>>(xn, wg, nullptr, g, S, F, D);
        gemm_k<<<dim3(F / 16, S / 16), 256, 0, stream>>>(xn, wu, nullptr, u, S, F, D);
        mul_k<<<S * F / 256, 256, 0, stream>>>(g, u, hh, S * F);
        u16* dst = l == 0 ? x2 : (u16*)d_out;
        gemm_k<<<dim3(D / 16, S / 16), 256, 0, stream>>>(hh, wd, x1, dst, S, D, F);
        x = dst;
    }
}
```

```cpp
#include <hip/hip_runtime.h>
#include <hip/hip_bf16.h>
#include <utility>

namespace mk {

typedef float    f32x16 __attribute__((ext_vector_type(16)));
typedef float    f32x4  __attribute__((ext_vector_type(4)));
typedef float    f32x2  __attribute__((ext_vector_type(2)));
typedef unsigned u32x4  __attribute__((ext_vector_type(4)));
typedef unsigned u32x2  __attribute__((ext_vector_type(2)));
typedef short    s16x4  __attribute__((ext_vector_type(4)));
typedef __bf16   bf16x2 __attribute__((ext_vector_type(2)));
typedef __amdgpu_buffer_rsrc_t srd_t;
#define DI __device__ __forceinline__
#define LAS __attribute__((address_space(3)))
#define GAP() __builtin_amdgcn_sched_barrier(0)
#define ACLOB "a0", "a1", "a2", "a3", "a4", "a5", "a6", "a7", "a8", "a9", "a10", "a11", "a12", "a13", "a14", "a15", "a16", "a17", "a18", "a19", "a20", "a21", "a22", "a23", "a24", "a25", "a26", "a27", "a28", "a29", "a30", "a31", "a32", "a33", "a34", "a35", "a36", "a37", "a38", "a39", "a40", "a41", "a42", "a43", "a44", "a45", "a46", "a47", "a48", "a49", "a50", "a51", "a52", "a53", "a54", "a55", "a56", "a57", "a58", "a59", "a60", "a61", "a62", "a63", "a64", "a65", "a66", "a67", "a68", "a69", "a70", "a71", "a72", "a73", "a74", "a75", "a76", "a77", "a78", "a79", "a80", "a81", "a82", "a83", "a84", "a85", "a86", "a87", "a88", "a89", "a90", "a91", "a92", "a93", "a94", "a95", "a96", "a97", "a98", "a99", "a100", "a101", "a102", "a103", "a104", "a105", "a106", "a107", "a108", "a109", "a110", "a111", "a112", "a113", "a114", "a115", "a116", "a117", "a118", "a119", "a120", "a121", "a122", "a123", "a124", "a125", "a126", "a127", "a128", "a129", "a130", "a131", "a132", "a133", "a134", "a135", "a136", "a137", "a138", "a139", "a140", "a141", "a142", "a143", "a144", "a145", "a146", "a147", "a148", "a149", "a150", "a151", "a152", "a153", "a154", "a155", "a156", "a157", "a158", "a159", "a160", "a161", "a162", "a163", "a164", "a165", "a166", "a167", "a168", "a169", "a170", "a171", "a172", "a173", "a174", "a175", "a176", "a177", "a178", "a179", "a180", "a181", "a182", "a183", "a184", "a185", "a186", "a187", "a188", "a189", "a190", "a191", "a192", "a193", "a194", "a195", "a196", "a197", "a198", "a199", "a200", "a201", "a202", "a203", "a204", "a205", "a206", "a207", "a208", "a209", "a210", "a211", "a212", "a213", "a214", "a215", "a216", "a217", "a218", "a219", "a220", "a221", "a222", "a223", "a224", "a225", "a226", "a227", "a228", "a229", "a230", "a231", "a232", "a233", "a234", "a235", "a236", "a237", "a238", "a239", "a240", "a241", "a242", "a243", "a244", "a245", "a246", "a247", "a248", "a249", "a250", "a251", "a252", "a253", "a254", "a255"
template <int... I> using iseq = std::integer_sequence<int, I...>;
template <int N> using mkseq = std::make_integer_sequence<int, N>;

constexpr unsigned AUX_SC1 = 16;
constexpr unsigned CH = 20480, NSLOT = 6, RING = CH * NSLOT;
constexpr unsigned LDS_BYTES = 163840;
constexpr unsigned L_CSQ = RING;
constexpr unsigned L_CSG = RING + 512;
constexpr unsigned L_AM  = RING + 2048;
constexpr unsigned L_KV  = RING + 2048 + 6656;
constexpr unsigned L_XCH = L_KV;
static_assert(L_KV + 128 * 176 <= LDS_BYTES, "lds");

constexpr unsigned W_EPOCH = 0, W_ERR = 64;
constexpr unsigned W_CSQ = 49152;
constexpr unsigned W_CSG = W_CSQ + 8 * 2048 * 4;
constexpr unsigned W_KVP = 0x30000;
constexpr unsigned KVP_H = 1920, KVP_C = 2 * KVP_H, KVP_J = 16 * KVP_C, KVP_L = 16 * KVP_J;
constexpr unsigned STL = 2048 * 64;
constexpr unsigned W_ST1 = 0x220000;
constexpr unsigned W_STI = W_ST1 + 2 * STL;
constexpr unsigned W_WQ  = 0x2A0000;
constexpr unsigned W_WO  = W_WQ + 16 * 163840;
constexpr unsigned W_WGU = W_WO + 8 * 102400;
constexpr unsigned W_WD  = W_WGU + 16 * 286720;
constexpr unsigned XT = 40960, XL = 64 * XT;
constexpr unsigned HT = 110592, HL = 64 * HT;
constexpr unsigned W_O   = W_WD + 8 * 286720;
constexpr unsigned W_X1  = W_O + 2 * XL;
constexpr unsigned W_XI  = W_X1 + 2 * XL;
constexpr unsigned W_H   = W_XI + 2 * XL;
constexpr unsigned W_DBG = W_H + 2 * HL;
constexpr unsigned W_DBG2 = W_DBG + 256 * 64 * 8;
static_assert(W_CSG + 4 * 3456 * 4 <= W_KVP && W_KVP + 2 * KVP_L <= W_ST1 && W_STI + 2 * STL <= W_WQ, "ws map");

constexpr unsigned W_FLAG = W_DBG2 + 256 * 2 * 4 * 256;
constexpr unsigned FL(unsigned line) { return W_FLAG + line * 128; }
constexpr unsigned F_TO = 0;
constexpr unsigned F_TQ = 16;
constexpr unsigned F_XI = F_TQ + 256;
constexpr unsigned F_O  = F_XI + 512;
constexpr unsigned F_X1 = F_O + 512;
constexpr unsigned F_H  = F_X1 + 512;
constexpr unsigned F_KV = F_H + 1536;
constexpr unsigned W_END = FL(F_KV + 1024);
constexpr unsigned SPIN_CAP = 1u << 16;

DI bool all_lanes(bool c) { return __builtin_amdgcn_ballot_w64(!c) == 0ull; }
DI unsigned rfl(unsigned x) { return __builtin_amdgcn_readfirstlane(x); }
DI unsigned opq(unsigned s) { asm volatile("" : "+s"(s)); return s; }
DI unsigned cvtpk(float lo, float hi) { f32x2 v = {lo, hi}; return __builtin_bit_cast(unsigned, __builtin_convertvector(v, bf16x2)); }
DI bf16x2 asbf2(unsigned u) { return __builtin_bit_cast(bf16x2, u); }
DI float dot2(unsigned a, unsigned b, float c) { return __builtin_amdgcn_fdot2_f32_bf16(asbf2(a), asbf2(b), c, false); }
DI float bflo(unsigned u) { return __uint_as_float(u << 16); }
DI float bfhi(unsigned u) { return __uint_as_float(u & 0xffff0000u); }
DI float xsum32(float x) { auto r = __builtin_amdgcn_permlane32_swap(__float_as_uint(x), __float_as_uint(x), false, false); return __uint_as_float(r[0]) + __uint_as_float(r[1]); }
DI float xsum16(float x) { auto r = __builtin_amdgcn_permlane16_swap(__float_as_uint(x), __float_as_uint(x), false, false); return __uint_as_float(r[0]) + __uint_as_float(r[1]); }
DI srd_t mksrd(const void* p, unsigned bytes) { return __builtin_amdgcn_make_buffer_rsrc((void*)p, (short)0, (int)bytes, 0x00020000); }
DI void bar() { asm volatile("s_waitcnt lgkmcnt(0)\n\ts_barrier" ::: "memory"); }
DI void rawbar() { asm volatile("s_barrier" ::: "memory"); }
template <int N> DI void vmwait() { static_assert(N >= 0, "count"); asm volatile("s_waitcnt vmcnt(%0)" :: "i"(N > 63 ? 63 : N) : "memory"); }

constexpr int b0_of(int m) { return m <= 6 ? 7 * m : 48; }
constexpr int ba_of(int s) { return s < 24 ? b0_of(s & 7) + 2 * (s >> 3) : 14 * (s - 24) + 6; }

template <int A, int IMM = 0> DI void a_load4(srd_t srd, unsigned voff, unsigned soff) {
    asm volatile("buffer_load_dwordx4 a[%c3:%c4], %0, %1, %2 offen offset:%5 sc1" :: "v"(voff), "s"(srd), "s"(soff), "i"(A), "i"(A + 3), "i"(IMM) : "memory", ACLOB);
}
template <int A> DI void a_load1(srd_t srd, unsigned voff, unsigned soff) {
    asm volatile("buffer_load_dword a[%c3], %0, %1, %2 offen sc1" :: "v"(voff), "s"(srd), "s"(soff), "i"(A) : "memory", ACLOB);
}
template <int A> DI void a_load2(srd_t srd, unsigned voff, unsigned soff) {
    asm volatile("buffer_load_dwordx2 a[%c3:%c4], %0, %1, %2 offen sc1" :: "v"(voff), "s"(srd), "s"(soff), "i"(A), "i"(A + 1) : "memory", ACLOB);
}
template <int A> DI u32x4 a_read4() {
    u32x4 r;
    asm volatile("v_accvgpr_read_b32 %0, a[%c4]\n\tv_accvgpr_read_b32 %1, a[%c5]\n\tv_accvgpr_read_b32 %2, a[%c6]\n\tv_accvgpr_read_b32 %3, a[%c7]"
                 : "=v"(r.x), "=v"(r.y), "=v"(r.z), "=v"(r.w) : "i"(A), "i"(A + 1), "i"(A + 2), "i"(A + 3));
    return r;
}
template <int A> DI u32x2 a_read2() {
    u32x2 r;
    asm volatile("v_accvgpr_read_b32 %0, a[%c2]\n\tv_accvgpr_read_b32 %1, a[%c3]" : "=v"(r.x), "=v"(r.y) : "i"(A), "i"(A + 1));
    return r;
}

struct Ctx {
    srd_t ws;
    unsigned lane, l16, wv;
    unsigned c, j;
    unsigned tlo, thi;
};
DI void fresh(Ctx& k) { k.c = opq(k.c); k.j = opq(k.j); k.wv = opq(k.wv); asm volatile("" : "+v"(k.lane), "+v"(k.l16)); }

template <bool WG = true> DI void post(const Ctx& k, unsigned off) {
    if constexpr (WG) asm volatile("; post wg"); else asm volatile("; post wave");
    if (k.lane == 0) { u32x2 t = {k.tlo, k.thi}; __builtin_amdgcn_raw_buffer_store_b64(t, k.ws, off, 0, AUX_SC1); }
}
template <bool WG = true> DI void post16(const Ctx& k, unsigned off, unsigned stride) {
    if constexpr (WG) asm volatile("; post wg"); else asm volatile("; post wave");
    if (k.lane < 16) { u32x2 t = {k.tlo, k.thi}; __builtin_amdgcn_raw_buffer_store_b64(t, k.ws, k.lane * stride, off, AUX_SC1); }
}
DI void poll_at(const Ctx& k, unsigned vo) {
    for (unsigned spins = 0;; ++spins) {
        u32x2 v;
        asm volatile("buffer_load_dwordx2 %0, %1, %2, 0 offen sc1\n\ts_waitcnt vmcnt(0)" : "=&v"(v) : "v"(vo), "s"(k.ws) : "memory");
        if (all_lanes(v.x == k.tlo && v.y == k.thi)) break;
        if (spins > SPIN_CAP) { if (k.lane == 0) __builtin_amdgcn_raw_buffer_store_b32(vo, k.ws, W_ERR, 0, AUX_SC1); break; }
    }
}
DI void poll(const Ctx& k, unsigned off, unsigned n) { poll_at(k, off + (k.lane < n ? k.lane : 0u) * 8u); }
DI void publish(const Ctx& k, unsigned base) {
    vmwait<0>();
    bar();
    if (k.wv == 0) post16(k, base + k.j * 8u, 128u);
}

constexpr int SB1 = 0, SB2 = 2, SB3 = 1, SB4 = 3;
constexpr unsigned slot_addr(int base, int n) { return (unsigned)((base + n) % 6) * CH; }
DI unsigned cs_of(unsigned j) { return 4 * (j >> 3) + (j & 3); }
DI unsigned seg_wq(const Ctx& k)  { return W_WQ  + k.j * 163840u; }
DI unsigned seg_wo(const Ctx& k)  { return W_WO  + cs_of(k.j) * 102400u; }
DI unsigned seg_wgu(const Ctx& k) { return W_WGU + k.j * 286720u; }
DI unsigned seg_wd(const Ctx& k)  { return W_WD  + cs_of(k.j) * 286720u; }
struct Dma { unsigned va, vb, ma, mb; };
DI Dma mkdma(const Ctx& k) { Dma d; d.va = k.l16 + k.wv * 4096u; d.vb = k.l16 + 16384u + k.wv * 1024u; d.ma = opq(k.wv * 4096u); d.mb = opq(16384u + k.wv * 1024u); return d; }
DI void dma_m0(unsigned base, unsigned lit) { asm volatile("s_add_u32 m0, %0, %1" :: "s"(base), "s"(lit) : "scc", "memory"); }
template <unsigned LIT> DI void dma_m0i(unsigned base) { asm volatile("s_add_u32 m0, %0, %1" :: "s"(base), "i"(LIT) : "scc", "memory"); }
template <unsigned LIT> DI unsigned sadd(unsigned base) { unsigned t; asm volatile("s_add_u32 %0, %1, %2" : "=s"(t) : "s"(base), "i"(LIT) : "scc"); return t; }
template <int K, unsigned SLOT> DI void dma_req(const Ctx& k, const Dma& d, unsigned so) {
    if constexpr (K < 4) asm volatile("buffer_load_dwordx4 %0, %1, %2 offen offset:%3 sc1 lds ; ringwr %c4" :: "v"(d.va), "s"(k.ws), "s"(so), "i"(K * 1024), "i"(SLOT / CH) : "memory");
    else                 asm volatile("buffer_load_dwordx4 %0, %1, %2 offen sc1 lds ; ringwr %c3" :: "v"(d.vb), "s"(k.ws), "s"(so), "i"(SLOT / CH) : "memory");
}
template <unsigned SLOT> DI void dma_chunk(const Ctx& k, const Dma& d, unsigned so) {
    dma_m0i<SLOT>(d.ma); asm volatile("s_nop 0");
    dma_req<0, SLOT>(k, d, so); dma_req<1, SLOT>(k, d, so); dma_req<2, SLOT>(k, d, so); dma_req<3, SLOT>(k, d, so);
    dma_m0i<SLOT>(d.mb); asm volatile("s_nop 0");
    dma_req<4, SLOT>(k, d, so);
}
template <unsigned SLOT> DI void dma_bulk(const Ctx& k, unsigned src) {
    if (k.wv >= 1) {
#pragma unroll 1
        for (unsigned i = k.wv - 1; i < 20; i += 3) {
            const unsigned m0v = SLOT + i * 1024u, so = src + i * 1024u;
            asm volatile("s_mov_b32 m0, %0\n\ts_nop 0\n\tbuffer_load_dwordx4 %1, %2, %3 offen sc1 lds ; ringwr %c4" :: "s"(m0v), "v"(k.l16), "s"(k.ws), "s"(so), "i"(SLOT / CH) : "memory");
        }
    }
}
template <unsigned ADDR> DI void wread_at(u32x4& w, unsigned vlo, unsigned vhi) {
    if constexpr (ADDR < 61440) asm volatile("ds_read_b128 %0, %1 offset:%2 ; ringrd %c3" : "=v"(w) : "v"(vlo), "i"(ADDR), "i"(ADDR / CH) : "memory");
    else                        asm volatile("ds_read_b128 %0, %1 offset:%2 ; ringrd %c3" : "=v"(w) : "v"(vhi), "i"(ADDR - 61440), "i"(ADDR / CH) : "memory");
}
template <int OFF> DI void wread(u32x4& w, unsigned vaddr) { asm volatile("ds_read_b128 %0, %1 offset:%2" : "=v"(w) : "v"(vaddr), "i"(OFF) : "memory"); }

enum { SEG_CUR, SEG_NXT };
struct Refill { int f0; unsigned slot; int seg; int n; bool counted; int st; };
constexpr int LA = 5;
constexpr int NI = 6;
constexpr int WIN = 16;
struct Rd { u32x4 w[8]; unsigned vlo, vhi; };
struct Act16 { unsigned vp[2], vs[2], vq[2][2][2], so; };
struct Segs { unsigned cur, nxt, so; bool on; };

template <class P> constexpr int refill_at(int f) { for (int i = 0; i < P::NR; ++i) { const int d = f - P::R[i].f0; if (d >= 0 && d % P::R[i].st == 0 && d / P::R[i].st < 5) return i; } return -1; }
template <class P> constexpr int refill_setup(int f) { for (int i = 0; i < P::NR; ++i) if (f == P::R[i].f0 - 1) return i; return -1; }
template <class P> constexpr int dma_counted(int f) { return refill_at<P>(f) >= 0 && P::R[refill_at<P>(f) >= 0 ? refill_at<P>(f) : 0].counted ? 1 : 0; }
template <class P> constexpr int refill_last(int n) { for (int i = 0; i < P::NR; ++i) if (P::R[i].seg == SEG_CUR && P::R[i].n == n) return P::R[i].f0 + 4 * P::R[i].st; return -1; }
template <class P, int F> DI void refill_gap(const Ctx& k, const Dma& d, Segs& sg) {
    constexpr int a = refill_at<P>(F), s = refill_setup<P>(F);
    if constexpr (a >= 0) {
        constexpr Refill R = P::R[a]; constexpr int K = (F - R.f0) / R.st;
        if (R.counted || sg.on) { dma_req<K, R.slot>(k, d, sg.so); }
        if constexpr (K == 3) dma_m0i<R.slot>(d.mb);
    }
    if constexpr (s >= 0) {
        constexpr Refill R = P::R[s];
        sg.so = sadd<R.n * CH>(R.seg == SEG_CUR ? sg.cur : sg.nxt);
        dma_m0i<R.slot>(d.ma);
    }
}

constexpr int areg_down(int s) { return s < 12 ? 160 + 8 * s : 8 * (s - 12); }
template <int A, unsigned LIT, int IMM> DI void a_load4s(const Ctx& k, unsigned voff, unsigned base) {
    unsigned t;
    asm volatile("s_add_u32 %0, %3, %6\n\tbuffer_load_dwordx4 a[%c4:%c5], %1, %2, %0 offen offset:%7 sc1"
                 : "=&s"(t) : "v"(voff), "s"(k.ws), "s"(base), "i"(A), "i"(A + 3), "i"(LIT), "i"(IMM) : "memory", "scc", ACLOB);
}
constexpr bool o_split(int s) { return s == 1 || s == 3 || s == 6 || s == 8; }
template <bool DOWN, int S, int T> DI void act16_load(const Ctx& k, const Act16& a) {
    if constexpr (DOWN) {
        if constexpr (S < 24) a_load4s<areg_down(S) + 4 * T, ba_of(S) * 1024u, 0>(k, a.vp[T], a.so);
        else                  a_load4s<areg_down(S) + 4 * T, ba_of(S) * 1024u, 0>(k, a.vs[T], a.so);
    } else if constexpr (o_split(S)) {
        constexpr int W = (S == 1 || S == 6) ? 0 : 1;
        const unsigned so = sadd<(S >> 1) * 4096u + (S & 1) * 2048u>(a.so);
        a_load2<8 * S + 4 * T>(k.ws, a.vq[W][T][0], so); a_load2<8 * S + 4 * T + 2>(k.ws, a.vq[W][T][1], so);
    } else a_load4s<8 * S + 4 * T, (S >> 1) * 4096u, (S & 1) * 2048>(k, a.vp[T], a.so);
}
template <int KS> DI void act32_load(const Ctx& k, unsigned so) { a_load4s<4 * KS, (KS >> 2) * 4096u, (KS & 3) * 1024>(k, k.l16, so); }

template <int PH> struct Seq;
template <> struct Seq<1> {
    static constexpr int NF = 160, SB = SB1, NPOST = 12, STEP = 3;
    static constexpr int post_i(int f) { return f % 3 == 2 && f >= 8 && f < 44 ? (f - 8) / 3 : -1; }
    static constexpr int NR = 6;
    static constexpr Refill R[NR] = {{70, slot_addr(SB1, 6), SEG_CUR, 6, true, 2}, {80, slot_addr(SB1, 7), SEG_CUR, 7, true, 2},
        {90, slot_addr(SB2, 0), SEG_NXT, 0, true, 2}, {100, slot_addr(SB2, 1), SEG_NXT, 1, true, 2}, {110, slot_addr(SB2, 2), SEG_NXT, 2, true, 4},
        {130, slot_addr(SB2, 3), SEG_NXT, 3, true, 4}};
    static constexpr int FPOLL = 64, FCHK = 88;
    static constexpr int extra(int f) { return f == FPOLL ? 1 : 0; }
    static constexpr bool mid(int q) { return q < 7; }
    static constexpr bool pre_landed = false;
    static constexpr int ks(int f) { return f < 120 ? f / 3 : f - 120; }
    static constexpr int ai(int f) { return f < 120 ? f % 3 : 3; }
    static constexpr bool lead(int f) { return f < 120 && f % 3 == 0; }
    static constexpr int first(int s) { return 3 * s; }
};
template <> struct Seq<3> {
    static constexpr int NF = 280, SB = SB3, NPOST = 8, STEP = 2;
    static constexpr int post_i(int f) { return (f & 1) == 1 && f >= 21 && f < 37 ? (f - 21) / 2 : -1; }
    static constexpr int NR = 14;
    static constexpr Refill R[NR] = {{50, slot_addr(SB3, 5), SEG_CUR, 5, true, 2}, {70, slot_addr(SB3, 6), SEG_CUR, 6, true, 2}, {80, slot_addr(SB3, 7), SEG_CUR, 7, true, 2},
        {90, slot_addr(SB3, 8), SEG_CUR, 8, true, 2}, {100, slot_addr(SB3, 9), SEG_CUR, 9, true, 2}, {110, slot_addr(SB3, 10), SEG_CUR, 10, true, 4},
        {130, slot_addr(SB3, 11), SEG_CUR, 11, true, 4}, {150, slot_addr(SB3, 12), SEG_CUR, 12, true, 4}, {170, slot_addr(SB3, 13), SEG_CUR, 13, true, 4},
        {190, slot_addr(SB4, 0), SEG_NXT, 0, true, 4}, {210, slot_addr(SB4, 1), SEG_NXT, 1, true, 4}, {230, slot_addr(SB4, 2), SEG_NXT, 2, true, 2},
        {250, slot_addr(SB4, 3), SEG_NXT, 3, true, 4}, {270, slot_addr(SB4, 4), SEG_NXT, 4, true, 2}};
    static constexpr int FPOLL = 176, FCHK = 199, FPRE = 200, FPOLL1 = 276;
    static constexpr int store(int f) { return (f >= 80 && f < 240 && (f % 80 == 24 || f % 80 == 44)) || f == 262 || f == 278 ? 1 : 0; }
    static constexpr int pre16(int f) { return f >= FPRE && f < FPRE + 32 && (f & 1) == 0 ? 1 : 0; }
    static constexpr int extra(int f) { return store(f) + (f == FPOLL || f == FPOLL1 ? 1 : 0) + pre16(f); }
    static constexpr bool mid(int q) { return q == 1 || q >= 3; }
    static constexpr bool pre_landed = true;
    static constexpr int ks(int f) { return f < 240 ? (f % 80) >> 1 : f - 240; }
    static constexpr int ai(int f) { return f < 240 ? 2 * ((f / 80) & 1) + (f & 1) : 2; }
    static constexpr bool lead(int f) { return f < 80 && (f & 1) == 0; }
    static constexpr int first(int s) { return 2 * s; }
};
template <int PH> struct Mdl {
    using S = Seq<PH>;
    static constexpr int ks(int f) { return S::ks(f); }
    static constexpr int ai(int f) { return S::ai(f); }
    static constexpr bool lead(int f) { return S::lead(f); }
    static constexpr int dma(int f) { return dma_counted<S>(f); }
    static constexpr int act_cum(int f) {
        int n = NI;
        for (int i = 0; i < f; ++i) { const int s = i / S::STEP, r = i % S::STEP; if (s < 40 && r < 2) { const int lim = s + WIN + 1 < 40 ? s + WIN + 1 : 40; if (n < lim && (r == 0 || n < NI + 2 * (s + 1))) ++n; } }
        return n;
    }
    static constexpr int act(int f) { return act_cum(f + 1) - act_cum(f); }
    static constexpr int post(int f) { return S::post_i(f) >= 0 ? 1 : 0; }
    static constexpr int before(int f) { int n = NI; for (int i = 0; i < f; ++i) n += dma(i) + act(i) + post(i) + S::extra(i); return n; }
    static constexpr int idx_act(int s) { if (s < NI) return s; for (int f = 0; f < S::NF; ++f) if (act(f) && act_cum(f) == s) return before(f) + dma(f); return -1; }
    static constexpr int idx_post_last() { for (int f = 0; f < S::NF; ++f) if (S::post_i(f) == S::NPOST - 1) return before(f) + dma(f) + act(f); return -1; }
    static constexpr int n_act(int f) { return before(f) - 1 - idx_act(ks(f)); }
    static constexpr int n_ring(int f) {
        const int n = f / 20 + 1, l = refill_last<S>(n);
        if (PH == 3 && n >= 14) return before(f) - 1 - before(S::R[9 + n - 14].f0 + 4 * S::R[9 + n - 14].st);
        if (20 * n >= S::NF) return 63;
        if (l >= 0) return before(f) - 1 - before(l);
        if (PH == 3) return n == 2 ? before(f) : 63;
        return n >= 3 ? before(f) : before(f) + 5 * (5 - n);
    }
    static constexpr int idx_extra(int f) { return before(f) + dma(f) + act(f) + post(f); }
};
template <bool Z, int A> DI void mfma32(const Ctx& k, f32x16& acc, const u32x4& w, const u32x4& p1, const u32x4& p2) {
    if constexpr (Z) asm volatile("v_mfma_f32_32x32x16_bf16 %0, %1, a[%c2:%c3], 0" : "=&v"(acc) : "v"(w), "i"(A), "i"(A + 3), "v"(p1), "v"(p2));
    else             asm volatile("v_mfma_f32_32x32x16_bf16 %0, %1, a[%c2:%c3], %0" : "+v"(acc) : "v"(w), "i"(A), "i"(A + 3));
}
template <int N> DI void lgkw() { asm volatile("s_waitcnt lgkmcnt(%0)" :: "i"(N) : "memory"); }
constexpr int lgk_out(int f, int nf, int la) { const int last = f + la < nf - 1 ? f + la : nf - 1; const int need = f + 1 < nf - 1 ? f + 1 : nf - 1; return last - need; }
template <int PH, int F, class H> DI void gap32(const Ctx& k, const Dma& d, Segs& sg, Rd& s, f32x16 (&acc)[4], unsigned aso, H& hook) {
    using S = Seq<PH>; using M = Mdl<PH>;
    constexpr int G = F + LA;
    if constexpr (G < S::NF) wread_at<slot_addr(S::SB, G / 20) + (G % 20) * 1024>(s.w[G & 7], s.vlo, s.vhi);
    if constexpr (M::lead(F)) vmwait<M::n_act(F)>();
    if constexpr ((F & 1) == 0) lgkw<lgk_out(F, S::NF, LA)>();
    mfma32<M::ks(F) == 0, 4 * M::ks(F)>(k, acc[M::ai(F)], s.w[F & 7], s.w[F >= 1 ? (F - 1) & 7 : 0], s.w[F >= 2 ? (F - 2) & 7 : 0]);
    if constexpr (F % 20 == 9 && S::mid(F / 20)) {
        constexpr int nr = M::n_ring(F), nh = H::template cover<F>();
        if constexpr ((nr < nh ? nr : nh) < 63) vmwait<(nr < nh ? nr : nh)>();
        rawbar();

    }
    refill_gap<S, F>(k, d, sg);
    if constexpr (M::act(F)) act32_load<M::act_cum(F)>(k, aso);
    if constexpr (M::post(F)) hook.template post_load<S::post_i(F)>(k);
    hook.template run<F>(k, acc);
    GAP();
}
template <int PH, int F0, class H, int... I> DI void run32_(const Ctx& k, const Dma& d, Segs& sg, Rd& s, f32x16 (&acc)[4], unsigned aso, H& hook, iseq<I...>) { (gap32<PH, F0 + I>(k, d, sg, s, acc, aso, hook), ...); }
template <int PH, int F0, int F1, class H> DI void run32(const Ctx& k, const Dma& d, Segs& sg, Rd& s, f32x16 (&acc)[4], unsigned aso, H& hook) { run32_<PH, F0>(k, d, sg, s, acc, aso, hook, mkseq<F1 - F0>{}); }
template <int... KS> DI void act32_init(const Ctx& k, unsigned so, iseq<KS...>) { (act32_load<KS>(k, so), ...); }
template <int PH, int... I> DI void wread_init32(Rd& s, iseq<I...>) { (wread_at<slot_addr(Seq<PH>::SB, 0) + I * 1024>(s.w[I], s.vlo, s.vhi), ...); }
template <int PH> DI void start32(const Ctx& k, Rd& s) {
    s.vlo = k.l16; s.vhi = k.l16 + 61440u;
    if constexpr (!Seq<PH>::pre_landed) {
        vmwait<NI + 25>();
        rawbar();
    }
    wread_init32<PH>(s, mkseq<LA>{});
}
DI void mfma_done() { asm volatile("s_nop 7\n\ts_nop 3" ::: "memory"); }
DI void pin(f32x16& a) { asm volatile("" : "+v"(a)); }

constexpr int LB = 5;
struct Res { srd_t srd; unsigned vo, so, stride; };
template <bool DOWN> struct P16;
template <> struct P16<false> {
    static constexpr int NS = 10, PRE = 4, SB = SB2, NINIT = 2 * PRE + 4;
    static constexpr int NR = 2;
    static constexpr Refill R[NR] = {{25, slot_addr(SB3, 0), SEG_NXT, 0, true, 2}, {35, slot_addr(SB3, 1), SEG_NXT, 1, true, 2}};
    static constexpr int FC0 = -1, FP1 = -1, FC1 = -1;
    static constexpr bool mid(int u) { return u == 3; }
    static constexpr int act_i(int f) { return f < 12 ? 8 + f : -1; }
};
template <> struct P16<true> {
    static constexpr int NS = 27, PRE = 8, SB = SB4, NINIT = 0;
    static constexpr int NR = 12;
    static constexpr Refill R[NR] = {{15, slot_addr(SB4, 6), SEG_CUR, 6, true, 2}, {25, slot_addr(SB4, 7), SEG_CUR, 7, true, 2}, {35, slot_addr(SB4, 8), SEG_CUR, 8, true, 2},
        {45, slot_addr(SB4, 9), SEG_CUR, 9, true, 2}, {55, slot_addr(SB4, 10), SEG_CUR, 10, true, 2}, {65, slot_addr(SB4, 11), SEG_CUR, 11, true, 2},
        {75, slot_addr(SB4, 12), SEG_CUR, 12, true, 2}, {85, slot_addr(SB4, 13), SEG_CUR, 13, true, 2},
        {95, slot_addr(SB1, 5), SEG_NXT, 5, false, 2}, {105, slot_addr(SB1, 0), SEG_NXT, 0, false, 2}, {115, slot_addr(SB1, 1), SEG_NXT, 1, false, 2}, {125, slot_addr(SB1, 2), SEG_NXT, 2, false, 2}};
    static constexpr int FC0 = 0, FP1 = 50, FC1 = 58;
    static constexpr bool mid(int u) { return u >= 1 && u <= 12; }
    static constexpr int act_i(int f) { return f < 16 ? 16 + f : (f >= FC1 && f < FC1 + 22) ? 32 + f - FC1 : -1; }
};
template <bool DOWN> struct M16 {
    using P = P16<DOWN>;
    static constexpr int FR = 12;
    static constexpr int areg(int s) { return DOWN ? areg_down(s) : 8 * s; }
    static constexpr int dma(int f) { return dma_counted<P>(f); }
    static constexpr int nops(int i) { return !DOWN && o_split(i >> 1) ? 2 : 1; }
    static constexpr int act(int f) { return P::act_i(f) >= 0 ? nops(P::act_i(f)) : 0; }
    static constexpr int act_gap(int i) { for (int f = 0; f < 5 * P::NS; ++f) if (P::act_i(f) == i) return f; return -1; }
    static constexpr int res(int f) { return f >= FR && f < FR + 5 ? 1 : 0; }
    static constexpr int pol(int f) { return f == P::FP1 ? 1 : 0; }
    static constexpr int before(int f) { int n = P::NINIT; for (int i = 0; i < f; ++i) n += dma(i) + act(i) + res(i) + pol(i); return n; }
    static constexpr int idx_act(int s) {
        if (s < P::PRE) { if (DOWN) return -1; int n = 0; for (int i = 0; i <= 2 * s + 1; ++i) n += nops(i); return n - 1; }
        const int f = act_gap(2 * s + 1); return before(f) + dma(f) + nops(2 * s + 1) - 1;
    }
    static constexpr int idx_pol(int f) { return before(f) + dma(f) + act(f) + res(f); }
    static constexpr int idx_res_last() { return before(FR + 4) + dma(FR + 4) + act(FR + 4); }
    static constexpr int n_ring(int f) { const int u = f / 10, l = refill_last<P>(u + 1); return !DOWN ? 63 : u == 1 ? before(f) + 5 : u == 2 ? before(f) : l >= 0 ? before(f) - 1 - before(l) : 63; }
};
template <bool Z, int A> DI void mfma16(const Ctx& k, f32x4& acc, const u32x4& w, const u32x4& p1) {
    if constexpr (Z) asm volatile("v_mfma_f32_16x16x32_bf16 %0, %1, a[%c2:%c3], 0" : "=&v"(acc) : "v"(w), "i"(A), "i"(A + 3), "v"(p1));
    else             asm volatile("v_mfma_f32_16x16x32_bf16 %0, %1, a[%c2:%c3], %0" : "+v"(acc) : "v"(w), "i"(A), "i"(A + 3));
}
template <int A> DI bool flags_ok(const Ctx& k) { const u32x2 v = a_read2<A>();  return all_lanes(v.x == k.tlo && v.y == k.thi); }
template <bool DOWN> constexpr unsigned w16_addr(int g) { return slot_addr(P16<DOWN>::SB, g / 10) + ((g % 10) / 5) * 10240 + (g % 5) * 1024; }
template <bool DOWN, int F> DI void gap16(const Ctx& k, const Dma& d, Segs& sg, Rd& s, const Act16& a, const Res& res, unsigned fh, f32x4 (&acc)[5][2]) {
    using P = P16<DOWN>; using M = M16<DOWN>;
    const unsigned fh0 = fh - k.j * 128u;
    constexpr int NF = 5 * P::NS, st = F / 5, nb = F % 5, G = F + LB;
    if constexpr (F == P::FC0) {
        vmwait<M::before(F) + 5>();
        if (!flags_ok<238>(k)) poll(k, fh + 256 * 128u, 16);
    }
    if constexpr (F == P::FC1) {
        vmwait<M::before(F) - 1 - M::idx_pol(P::FP1)>();
        if (!flags_ok<130>(k)) poll(k, fh + 2 * 256 * 128u, 16);
    }
    if constexpr (nb == 0 && M::idx_act(st) >= 0) vmwait<M::before(F) - 1 - M::idx_act(st)>();
    if constexpr (G < NF) wread_at<w16_addr<DOWN>(G)>(s.w[G & 7], s.vlo, s.vhi);
    if constexpr ((F & 1) == 0) lgkw<lgk_out(F, NF, LB)>();
    mfma16<st == 0, M::areg(st)>(k, acc[nb][0], s.w[F & 7], s.w[F >= 1 ? (F - 1) & 7 : 0]);
    mfma16<st == 0, M::areg(st) + 4>(k, acc[nb][1], s.w[F & 7], s.w[F >= 1 ? (F - 1) & 7 : 0]);
    if constexpr (F % 10 == 4 && P::mid(F / 10)) {
        if constexpr (M::n_ring(F) < 63) vmwait<M::n_ring(F)>();
        rawbar();
        if constexpr (DOWN && F == 14) { if (k.wv == 0) post16(k, fh0 + 2 * 256 * 128u + k.j * 8u, 128u); }

    }
    refill_gap<P, F>(k, d, sg);
    if constexpr (M::act(F)) act16_load<DOWN, (P::act_i(F) >> 1), (P::act_i(F) & 1)>(k, a);
    if constexpr (M::res(F)) a_load2<120 + 2 * (F - M::FR)>(res.srd, res.vo, sadd<(F - M::FR) * 1024u>(res.so));
    if constexpr (M::pol(F)) a_load2<130>(k.ws, (k.lane & 15) * 8u, fh + 2 * 256 * 128u);
    GAP();
}
template <bool DOWN, int... F> DI void pass16_(const Ctx& k, const Dma& d, Segs& sg, Rd& s, const Act16& a, const Res& res, unsigned fh, f32x4 (&acc)[5][2], iseq<F...>) { (gap16<DOWN, F>(k, d, sg, s, a, res, fh, acc), ...); }
template <bool DOWN, int... I> DI void act16_init(const Ctx& k, const Act16& a, iseq<I...>) { (act16_load<DOWN, (I >> 1), (I & 1)>(k, a), ...); }
template <bool DOWN, int... I> DI void wread_init16(Rd& s, iseq<I...>) { (wread_at<w16_addr<DOWN>(I)>(s.w[I], s.vlo, s.vhi), ...); }
template <bool DOWN> DI void pass16(const Ctx& k, const Dma& d, Segs& sg, const Act16& a, const Res& res, unsigned fh, f32x4 (&acc)[5][2]) {
    Rd s; s.vlo = k.l16 + (k.wv >> 1) * 5120u; s.vhi = s.vlo + 61440u;
    if constexpr (!DOWN) act16_init<false>(k, a, mkseq<2 * P16<false>::PRE>{});
    wread_init16<DOWN>(s, mkseq<LB>{});
    pass16_<DOWN>(k, d, sg, s, a, res, fh, acc, mkseq<5 * P16<DOWN>::NS>{});
    vmwait<M16<DOWN>::before(5 * P16<DOWN>::NS) - 1 - M16<DOWN>::idx_res_last()>();
    asm volatile("s_nop 7\n\ts_nop 1" : "+v"(acc[4][0]), "+v"(acc[4][1]), "+v"(acc[3][0]), "+v"(acc[3][1]), "+v"(acc[2][0]), "+v"(acc[2][1]), "+v"(acc[1][0]), "+v"(acc[1][1]), "+v"(acc[0][0]), "+v"(acc[0][1]));
}

DI void raw_dma(srd_t srd, unsigned m0v, unsigned voff, unsigned soff) {
    asm volatile("s_mov_b32 m0, %0\n\ts_nop 0\n\tbuffer_load_dwordx4 %1, %2, %3 offen lds" :: "s"(m0v), "v"(voff), "s"(srd), "s"(soff) : "memory");
}
DI u32x4 tr_frag(const LAS char* lds, unsigned a0, unsigned a1) {
    const s16x4 lo = __builtin_amdgcn_ds_read_tr16_b64_v4i16((LAS s16x4*)(lds + a0));
    const s16x4 hi = __builtin_amdgcn_ds_read_tr16_b64_v4i16((LAS s16x4*)(lds + a1));
    const u32x2 l = __builtin_bit_cast(u32x2, lo), h = __builtin_bit_cast(u32x2, hi);
    return (u32x4){l.x, l.y, h.x, h.y};
}
DI float colsum8(const u32x4& v, float acc) {
    const unsigned one = 0x3f803f80u, x = v.x, y = v.y, z = v.z, w = v.w;
    return dot2(w, one, dot2(z, one, dot2(y, one, dot2(x, one, acc))));
}
struct Ptrs { const void *x0, *wqkv, *wo, *wg, *wu, *wd; void* out; void* ws; };
constexpr unsigned L_XS = L_KV, L_QS = L_KV + 10240;

template <int PART> DI void joba(const Ctx& k, const Ptrs& p, const LAS char* lds) {
    const unsigned l = k.lane;
    if (k.wv == 0) {
        const srd_t s = mksrd(p.x0, 2048 * 640 * 2);
        const unsigned row0 = 128 * k.c + 32 * (k.j >> 2), q = k.j & 3, t = l & 31, h = l >> 5, tb = 4 * k.c + (k.j >> 2);
        if constexpr (PART == 0) {
#pragma unroll
            for (unsigned i = 0; i < 10; ++i) { const unsigned n = 64 * i + l, row = n / 20u, ch = n - 20u * row;
                raw_dma(s, L_XS + i * 1024u, (row0 + row) * 1280u + ch * 16u, q * 320u); }
            return;
        }
        u32x4 v[10];
#pragma unroll
        for (int i = 0; i < 10; ++i) {
            const unsigned a = L_XS + t * 320u + i * 32u + h * 8u;
            const u32x2 lo = *(const LAS u32x2*)(lds + a), hi = *(const LAS u32x2*)(lds + a + 16u);
            v[i] = (u32x4){lo.x, lo.y, hi.x, hi.y};
        }
#pragma unroll
        for (int i = 0; i < 10; ++i) __builtin_amdgcn_raw_buffer_store_b128(v[i], k.ws, k.l16, W_XI + tb * XT + (10 * q + i) * 1024u, AUX_SC1);
        float s1 = 0.f, sq = 0.f;
#pragma unroll
        for (int i = 0; i < 10; ++i) {
            s1 = colsum8(v[i], s1);
            { const unsigned x = v[i].x, y = v[i].y, z = v[i].z, u = v[i].w; sq = dot2(u, u, dot2(z, z, dot2(y, y, dot2(x, x, sq)))); }
        }
        s1 = xsum32(s1); sq = xsum32(sq);
        __builtin_amdgcn_raw_buffer_store_b64(h == 0 ? (u32x2){__float_as_uint(s1), __float_as_uint(sq)} : (u32x2){0u, 0u}, k.ws, k.lane * 8u, W_STI + tb * 2048u + q * 512u, AUX_SC1);
    } else if (k.wv == 1) {
        const srd_t s = mksrd(p.wqkv, 640 * 1920 * 2);
        const unsigned ch = 8 * (k.c & 1) + (l & 7), j = k.j;
        const unsigned col = ch < 5 ? 640 + 40 * j + 8 * ch : ch < 10 ? 1280 + 40 * j + 8 * (ch - 5) : 40 * j + 8 * (ch - 10);
        const unsigned vo = ch == 15 ? 0x40000000u : (l >> 3) * 3840u + col * 2u;
        if constexpr (PART == 0) {
#pragma unroll
            for (unsigned i = 0; i < 10; ++i) raw_dma(s, L_QS + i * 1024u, vo, (80 * (k.c >> 1) + 8 * i) * 3840u);
            return;
        }
        const unsigned lam = l & 15, qq = lam >> 2, pp = lam & 3, grp = l >> 4, hh = grp >> 1, nh = grp & 1, ks0 = 5 * (k.c >> 1);
        const unsigned a = L_QS + (4 * hh + qq) * 128u + (16 * nh + 4 * pp) * 2u;
        u32x4 v[2][5];
#pragma unroll
        for (int w = 0; w < 2; ++w)
#pragma unroll
            for (int i = 0; i < 5; ++i) v[w][i] = tr_frag(lds, a + w * 64u + i * 2048u, a + w * 64u + i * 2048u + 1024u);
#pragma unroll
        for (int w = 0; w < 2; ++w) {
            const unsigned nb = 2 * (k.c & 1) + w;
#pragma unroll
            for (int i = 0; i < 5; ++i)
                __builtin_amdgcn_raw_buffer_store_b128(v[w][i], k.ws, k.l16, W_WQ + k.j * 163840u + (nb < 3 ? ((ks0 + i) * 3 + nb) * 1024u : 122880u + (ks0 + i) * 1024u), AUX_SC1);
        }
#pragma unroll
        for (int w = 0; w < 2; ++w) {
            float cs = 0.f;
#pragma unroll
            for (int i = 0; i < 5; ++i) cs = colsum8(v[w][i], cs);
            cs = xsum32(cs);
            if (l < 32) __builtin_amdgcn_raw_buffer_store_b32(__float_as_uint(cs), k.ws, l * 4u, W_CSQ + ((k.c >> 1) * 2048 + k.j * 128 + 32 * (2 * (k.c & 1) + w)) * 4u, AUX_SC1);
        }
    }
}
DI void joba_post(const Ctx& k) {
    vmwait<0>();
    post16<false>(k, k.wv == 0 ? FL(F_XI + k.c * 16) + k.j * 8u : FL(F_TQ + k.j * 16) + k.c * 8u, 128u);
}
DI void jobb_issue(const Ctx& k, const Ptrs& p, unsigned b) {
    const unsigned l = k.lane, w = k.wv & 1;
    if (b < 108) {
        const unsigned q = b >> 2, qt = b & 3;
        const srd_t s = mksrd(w == 0 ? p.wg : p.wu, 640 * 1728 * 2);
        const unsigned vo = (l >> 3) * 3456u + (l & 7) * 16u;
#pragma unroll 5
        for (unsigned i = 0; i < 20; ++i) raw_dma(s, w * 20480u + i * 1024u, vo, (160 * qt + 8 * i) * 3456u + q * 128u);
    } else if (b < 162) {
        const unsigned idx = b - 108, kh = idx / 27u, sg = idx - 27u * kh;
        const unsigned m = sg & 7, ba = 54 * kh + (sg < 24 ? (m <= 6 ? 7 * m : 48u) + 2 * (sg >> 3) : 14 * (sg - 24) + 6), bb = ba + (sg < 24 ? 1u : 7u);
        const srd_t s = mksrd(p.wd, 1728 * 640 * 2);
#pragma unroll 5
        for (unsigned i = 20 * w; i < 20 * w + 20; ++i) { const unsigned n = 64 * i + l, row = n / 80u, ch = n - 80u * row;
            raw_dma(s, i * 1024u, (16 * (row < 16 ? ba : bb) + (row & 15)) * 1280u + ch * 16u, 0); }
    } else if (b < 194) {
        const unsigned idx = b - 162, cs = idx >> 2, kh = (idx >> 1) & 1, hf = idx & 1;
        const srd_t s = mksrd(p.wo, 640 * 640 * 2);
#pragma unroll 1
        for (unsigned i = w; i < 25; i += 2) { const unsigned n = 64 * i + l, row = n / 10u, ch = n - 10u * row;
            raw_dma(s, i * 1024u, row * 1280u + ch * 16u, ((320 * kh + 160 * hf) * 640 + 80 * cs) * 2u); }
    }
}
DI void jobb_emit(const Ctx& k, const LAS char* lds, unsigned b) {
    const unsigned l = k.lane, w = k.wv, lam = l & 15, qq = lam >> 2, pp = lam & 3, grp = l >> 4;
    if (b >= 162) {
        if (b < 194) {
            const unsigned idx = b - 162, cs = idx >> 2, kh = (idx >> 1) & 1, hf = idx & 1;
            const unsigned a = (16 * (grp >> 1) + 4 * (grp & 1) + qq) * 160u + 8 * pp;
#pragma unroll 1
            for (unsigned f = w; f < 25; f += 4) { const unsigned sl = f / 5u, nb = f - 5u * sl, sg = 5 * hf + sl;
                const unsigned aa = a + sl * 5120u + nb * 32u;
                const u32x4 v = tr_frag(lds, aa, aa + 8 * 160u);
                __builtin_amdgcn_raw_buffer_store_b128(v, k.ws, k.l16, W_WO + cs * 102400u + (sg >> 1) * CH + ((sg & 1) * 2 + kh) * 5120u + nb * 1024u, AUX_SC1); }
        }
    } else if (b < 108) {
        const unsigned q = b >> 2, qt = b & 3, hh = grp >> 1, mat = grp & 1;
        const unsigned B = 4 * q + w, side = B / 54u, rr = B - 54u * side, m = rr < 42 ? rr / 7u : 6 + (rr - 42) / 6u, bl = rr - (m <= 6 ? 7 * m : 48u), j = 8 * side + m;
        const unsigned a = mat * 20480u + (4 * hh + qq) * 128u + (16 * w + 4 * pp) * 2u;
        const unsigned dst = W_WGU + j * 286720u + (bl < 6 ? (bl >> 1) * 81920u + (bl & 1) * 1024u : 245760u), kst = bl < 6 ? 2048u : 1024u;
        float cs = 0.f;
#pragma unroll
        for (int h5 = 0; h5 < 2; ++h5) {
            u32x4 v[5];
#pragma unroll
            for (int i = 0; i < 5; ++i) v[i] = tr_frag(lds, a + (5 * h5 + i) * 2048u, a + (5 * h5 + i) * 2048u + 1024u);
#pragma unroll
            for (int i = 0; i < 5; ++i) { cs = colsum8(v[i], cs); __builtin_amdgcn_raw_buffer_store_b128(v[i], k.ws, k.l16, dst + (10 * qt + 5 * h5 + i) * kst, AUX_SC1); }
        }
        cs = xsum32(cs);
        if (l < 32) __builtin_amdgcn_raw_buffer_store_b32(__float_as_uint(cs), k.ws, l * 4u, W_CSG + (qt * 3456 + B * 32) * 4u, AUX_SC1);
    } else {
        const unsigned idx = b - 108, kh = idx / 27u, sg = idx - 27u * kh;
        const unsigned a = (16 * (grp >> 1) + 4 * (grp & 1) + qq) * 1280u + 8 * pp + 320u * w;
        const unsigned dst = W_WD + 2 * w * 286720u + (sg >> 1) * CH + ((sg & 1) * 2 + kh) * 5120u;
#pragma unroll
        for (int c2 = 0; c2 < 2; ++c2) {
            u32x4 v[5];
#pragma unroll
            for (int nb = 0; nb < 5; ++nb) v[nb] = tr_frag(lds, a + c2 * 160u + nb * 32u, a + c2 * 160u + nb * 32u + 8 * 1280u);
#pragma unroll
            for (int nb = 0; nb < 5; ++nb) __builtin_amdgcn_raw_buffer_store_b128(v[nb], k.ws, k.l16, dst + c2 * 286720u + nb * 1024u, AUX_SC1);
        }
    }
}

struct Stat { float rstd, nmean; };
DI void stat_issue(const Ctx& k, unsigned so) {
    const unsigned vo = (k.lane & 31) * 64u + (k.lane >> 5) * 32u;
    a_load4<248>(k.ws, vo, so); a_load4<252>(k.ws, vo + 16u, so);
}
constexpr int FS = 52, FS1 = 70;
struct StatSteps {
    u32x4 a, b; float s, q;
    template <int I> DI void step(const Ctx& k, Stat& st) {
        if constexpr (I == 0) { a = a_read4<248>();  }
        if constexpr (I == 1) { b = a_read4<252>();  }
        if constexpr (I == 2) { s = (__uint_as_float(a.x) + __uint_as_float(a.z)) + (__uint_as_float(b.x) + __uint_as_float(b.z)); asm volatile("" : "+v"(s)); }
        if constexpr (I == 3) { q = (__uint_as_float(a.y) + __uint_as_float(a.w)) + (__uint_as_float(b.y) + __uint_as_float(b.w)); asm volatile("" : "+v"(q)); }
        if constexpr (I == 4) { s = xsum32(s) * (1.f / 640.f); asm volatile("" : "+v"(s)); }
        if constexpr (I == 5) { q = xsum32(q) * (1.f / 640.f); asm volatile("" : "+v"(q)); }
        if constexpr (I == 6) { q = fmaxf(fmaf(-s, s, q), 0.f) + 1e-5f; asm volatile("" : "+v"(q)); }
        if constexpr (I == 7) { st.rstd = __builtin_amdgcn_rsqf(q); st.nmean = -s; asm volatile("" : "+v"(st.rstd), "+v"(st.nmean)); }
    }
};
template <int A, unsigned QS, int... Q> DI void cs_issue_(const Ctx& k, unsigned so, iseq<Q...>) { (a_load1<A + Q>(k.ws, (k.wv * 64 + k.lane) * 4u, sadd<Q * QS>(so)), ...); }
DI float sum4(const u32x4& a) { return (__uint_as_float(a.x) + __uint_as_float(a.y)) + (__uint_as_float(a.z) + __uint_as_float(a.w)); }
DI float phi1(float y1) { return __builtin_amdgcn_fmed3f(__builtin_amdgcn_exp2f(y1 * 1.44269504f), 0.f, 1.f) + fmaxf(y1, 1e-6f); }

DI float phi_fma(float a, float r, float c) {
    float y, e;
    asm volatile("v_fma_f32 %0, %2, %3, %4\n\tv_mul_f32 %1, 0x3fb8aa3b, %0\n\tv_exp_f32 %1, %1 clamp\n\tv_max_f32 %0, 0x358637bd, %0\n\tv_add_f32 %0, %0, %1"
                 : "=&v"(y), "=&v"(e) : "v"(a), "v"(r), "v"(c));
    return y;
}
struct Hook1 {
    LAS char* lds; Stat st; StatSteps ss; float nm; unsigned vcs, fto, cso, sto; LAS char* row;
    template <int I> DI void post_load(const Ctx& k) {
        if constexpr (I < 8) a_load1<240 + I>(k.ws, (k.wv * 64 + k.lane) * 4u, sadd<I * 2048 * 4u>(cso));
        else a_load2<248 + 2 * (I - 8)>(k.ws, (k.lane & 31) * 8u + (k.lane >> 5) * 1024u, sadd<(I - 8) * 256u>(sto));
    }
    u32x4 c[10], t0, t1; float y, p[4];
    using M = Mdl<1>; using S = Seq<1>;
    template <int F> static constexpr int cover() { return 63; }
    template <int F> DI void run(const Ctx& k, f32x16 (&acc)[4]) {
        if constexpr (F == FS1) vmwait<M::before(F) + M::dma(F) + M::act(F) + M::post(F) - 1 - M::idx_post_last()>();
        if constexpr (F >= FS1 && F < FS1 + 8) ss.template step<F - FS1>(k, st);
        if constexpr (F == FS1 + 8) { nm = st.nmean * st.rstd; asm volatile("" : "+v"(nm)); }
        if constexpr (F == FS1 + 10) { t0 = a_read4<240>();  }
        if constexpr (F == FS1 + 11) { t1 = a_read4<244>();  }
        if constexpr (F == FS1 + 12) { y = sum4(t0); asm volatile("" : "+v"(y)); }
        if constexpr (F == FS1 + 13) { *(LAS float*)(lds + L_CSQ + (k.wv * 64 + k.lane) * 4u) = y + sum4(t1); asm volatile("; tblwr 1" ::: "memory"); }
        if constexpr (F == 65) { if (k.lane < 32) *(LAS u32x2*)(row + 160) = (u32x2){0x00003f80u, 0u}; }
        if constexpr (F == S::FPOLL) a_load2<236>(k.ws, (k.lane & 3) * 8u, fto);
        if constexpr (F == S::FCHK) {
            vmwait<M::before(F) + M::dma(F) + M::act(F) - 1 - M::idx_extra(S::FPOLL)>();
            if (!flags_ok<236>(k)) poll(k, fto, 4);
        }
        if constexpr (F == 90) asm volatile("; tblrd 1" ::: "memory");
        if constexpr (F >= 90 && F < 100) wread<(F - 90) * 32>(c[F - 90], vcs);
        if constexpr (F >= 106 && F < 116) {
            constexpr int G = F - 106;
            asm volatile("" : "+v"(c[G]));
#pragma unroll
            for (int i = 0; i < 4; ++i) c[G][i] = __float_as_uint(fmaf(nm, __uint_as_float(c[G][i]), G < 5 ? 1e-6f : 0.f));
            asm volatile("" : "+v"(c[G]));
        }
        if constexpr (F >= 123 && F < 143) {
            constexpr int e = F - 123, G = e >> 2, i = e & 3;
            p[i] = phi_fma(acc[G >> 2][4 * (G & 3) + i], st.rstd, __uint_as_float(c[G][i]));
            if constexpr (i == 3) *(LAS u32x2*)(row + 16 * G) = (u32x2){cvtpk(p[0], p[1]), cvtpk(p[2], p[3])};
        }
        if constexpr (F >= 143 && F < 153) vgroup<5 + ((F - 143) >> 1), 2 * ((F - 143) & 1)>(acc);
    }
    template <int G, int i> DI void vgroup(f32x16 (&acc)[4]) {
        p[i]     = fmaf(acc[G >> 2][4 * (G & 3) + i],     st.rstd, __uint_as_float(c[G][i]));
        p[i + 1] = fmaf(acc[G >> 2][4 * (G & 3) + i + 1], st.rstd, __uint_as_float(c[G][i + 1]));
        asm volatile("" : "+v"(p[i]), "+v"(p[i + 1]));
        if constexpr (i == 2) *(LAS u32x2*)(row + 16 * G) = (u32x2){cvtpk(p[0], p[1]), cvtpk(p[2], p[3])};
    }
};
DI void mfma32v(f32x16& acc, const u32x4& a, const u32x4& b) { asm volatile("s_nop 1\n\tv_mfma_f32_32x32x16_bf16 %0, %1, %2, %0" : "+v"(acc) : "v"(a), "v"(b)); }
DI void mfma32z(f32x16& acc, const u32x4& a, const u32x4& b) { asm volatile("s_nop 1\n\tv_mfma_f32_32x32x16_bf16 %0, %1, %2, 0" : "=&v"(acc) : "v"(a), "v"(b)); }
template <int OFF> DI void tr_read(u32x2& v, unsigned addr) { asm volatile("ds_read_b64_tr_b16 %0, %1 offset:%2" : "=v"(v) : "v"(addr), "i"(OFF) : "memory"); }
template <int S> DI void kv_reads(u32x2 (&f)[8][4], unsigned pa, unsigned pb) {
    tr_read<S * 2816>(f[S][0], pa); tr_read<S * 2816 + 704>(f[S][1], pa); tr_read<S * 2816>(f[S][2], pb); tr_read<S * 2816 + 704>(f[S][3], pb);
}
template <int S> DI void kv_step(f32x16& d, u32x2 (&f)[8][4], unsigned pa, unsigned pb) {
    asm volatile("s_waitcnt lgkmcnt(%4)" : "+v"(f[S][0]), "+v"(f[S][1]), "+v"(f[S][2]), "+v"(f[S][3]) : "i"(S < 6 ? 8 : 4 * (7 - S)) : "memory");
    const u32x4 a = {f[S][0].x, f[S][0].y, f[S][1].x, f[S][1].y}, b = {f[S][2].x, f[S][2].y, f[S][3].x, f[S][3].y};
    if constexpr (S == 0) mfma32z(d, a, b); else mfma32v(d, a, b);
    if constexpr (S + 3 < 8) kv_reads<S + 3>(f, pa, pb);
}
DI void kv_partial(const Ctx& k, unsigned layer) {
    const unsigned l = k.lane, t = l & 31, h = l >> 5;
    const unsigned head = k.wv, lam = l & 15, qq = lam >> 2, pp = lam & 3, nh = (l >> 4) & 1, rc = 16 * nh + 4 * pp;
    const unsigned ca = 20 * head + (rc < 20 ? rc : 16u), cb = rc < 20 ? 40 + 20 * head + rc : 80u;
    const unsigned ra = L_KV + (8 * h + qq) * 176u, pa = ra + ca * 2u, pb = ra + cb * 2u;
    u32x2 f[8][4];
    kv_reads<0>(f, pa, pb); kv_reads<1>(f, pa, pb); kv_reads<2>(f, pa, pb);
    f32x16 d;
    kv_step<0>(d, f, pa, pb); kv_step<1>(d, f, pa, pb); kv_step<2>(d, f, pa, pb); kv_step<3>(d, f, pa, pb);
    kv_step<4>(d, f, pa, pb); kv_step<5>(d, f, pa, pb); kv_step<6>(d, f, pa, pb); kv_step<7>(d, f, pa, pb);
    asm volatile("s_nop 7\n\ts_nop 3" : "+v"(d));

    if (t < 24) {
        const unsigned so = W_KVP + layer * KVP_L + k.j * KVP_J + k.c * KVP_C + head * KVP_H, vo = (2 * t + h) * 16u;
        __builtin_amdgcn_raw_buffer_store_b128(__builtin_bit_cast(u32x4, (f32x4){d[0], d[1], d[2], d[3]}), k.ws, vo, so, AUX_SC1);
        __builtin_amdgcn_raw_buffer_store_b128(__builtin_bit_cast(u32x4, (f32x4){d[4], d[5], d[6], d[7]}), k.ws, vo, so + 768u, AUX_SC1);
        if (h == 0) __builtin_amdgcn_raw_buffer_store_b128(__builtin_bit_cast(u32x4, (f32x4){d[8], d[9], d[10], d[11]}), k.ws, t * 16u, so + 1536u, AUX_SC1);
    }
}
DI void q_epilogue(const Ctx& k, const LAS char* lds, const f32x16 (&acc)[4], const Stat st, u32x2 (&pq)[5]) {
    const unsigned h = k.lane >> 5;
    const float nm = st.nmean * st.rstd;
    f32x4 cs[5];
#pragma unroll
    for (int g = 0; g < 5; ++g) cs[g] = *(const LAS f32x4*)(lds + L_CSQ + (8 * (10 + g)) * 4 + 16u * h);
#pragma unroll
    for (int g = 0; g < 5; ++g) {
        const int G = 10 + g;
        float y[4];
#pragma unroll
        for (int i = 0; i < 4; ++i) y[i] = phi1(fmaf(acc[G >> 2][4 * (G & 3) + i], st.rstd, fmaf(nm, cs[g][i], 1e-6f)));
        pq[g] = (u32x2){cvtpk(y[0], y[1]), cvtpk(y[2], y[3])};
        asm volatile("" : "+v"(pq[g]));
    }
}

DI void p1b(const Ctx& k, LAS char* lds, unsigned layer, const u32x2 (&pq)[5]) {
    const unsigned tid = k.wv * 64 + k.lane, l = k.lane, h = l >> 5;
    if (tid < 210) {
        const unsigned so = W_KVP + layer * KVP_L + k.j * KVP_J;
        const unsigned head = tid >= 105, r = tid - 105 * head, g = r >= 42, r2 = r - 42 * g;
        const unsigned e = r < 84 ? r2 >> 1 : r - 84, dq = r < 84 ? 2 * g + (r2 & 1) : 4u;
        const unsigned vo = head * KVP_H + (r < 84 ? g * 768u + r2 * 16u : 1536u + e * 16u);
        u32x4 v[16];
#pragma unroll
        for (int c = 0; c < 16; ++c) v[c] = __builtin_amdgcn_raw_buffer_load_b128(k.ws, vo, so + c * KVP_C, AUX_SC1);
        const unsigned col = (20 * head + 4 * dq) * 2u;
        f32x4 a0 = __builtin_bit_cast(f32x4, v[0]), a1 = __builtin_bit_cast(f32x4, v[1]);
#pragma unroll
        for (int c = 2; c < 16; c += 2) { a0 += __builtin_bit_cast(f32x4, v[c]); a1 += __builtin_bit_cast(f32x4, v[c + 1]); }
        a0 += a1;
        asm volatile("" : "+v"(a0));

        const u32x2 pk = {cvtpk(a0[0], a0[1]), cvtpk(a0[2], a0[3])};
        if (e < 20) *(LAS u32x2*)(lds + L_AM + (20 * head + e) * 104u + col) = pk;
        else { *(LAS u32x2*)(lds + L_AM + (40 + head) * 104u + col) = pk; *(LAS u32x2*)(lds + L_AM + (44 + head) * 104u + col) = pk; }
    }
    bar();

    const LAS char* am = lds + L_AM + (l & 31) * 104u + 8u * h;
    u32x2 a[3][4];
#pragma unroll
    for (int s = 0; s < 3; ++s) {
        a[s][0] = *(const LAS u32x2*)(am + 32 * s); a[s][1] = *(const LAS u32x2*)(am + 32 * s + 16);
        a[s][2] = *(const LAS u32x2*)(am + 32 * 104 + 32 * s); a[s][3] = *(const LAS u32x2*)(am + 32 * 104 + 32 * s + 16);
    }
    f32x16 d0, d1;
#pragma unroll
    for (int s = 0; s < 3; ++s) {
        const u32x2 p0 = pq[2 * s], p1 = s < 2 ? pq[2 * s + 1] : (u32x2){0u, 0u};
        const u32x4 fb = {p0.x, p0.y, p1.x, p1.y};
        const u32x4 f0 = {a[s][0].x, a[s][0].y, a[s][1].x, a[s][1].y}, f1 = {a[s][2].x, a[s][2].y, a[s][3].x, a[s][3].y};
        if (s == 0) { mfma32z(d1, f1, fb); mfma32z(d0, f0, fb); } else { mfma32v(d1, f1, fb); mfma32v(d0, f0, fb); }
    }
    asm volatile("s_nop 7\n\ts_nop 3" : "+v"(d0), "+v"(d1));
    const float r0 = __builtin_amdgcn_rcpf(d1[4] + 1e-6f), r1 = __builtin_amdgcn_rcpf(d1[5] + 1e-6f);
    u32x2 o[5];
#pragma unroll
    for (int G = 0; G < 5; ++G) {
        const float rr = G < 2 ? r0 : G > 2 ? r1 : (h ? r1 : r0);
        float v[4];
#pragma unroll
        for (int i = 0; i < 4; ++i) v[i] = (G < 4 ? d0[4 * G + i] : d1[i]) * rr;
        o[G] = (u32x2){cvtpk(v[0], v[1]), cvtpk(v[2], v[3])};
    }
    const unsigned so = W_O + layer * XL + (4 * k.c + k.wv) * XT + ((5 * k.j) >> 1) * 1024u;
    if (k.j & 1) {
        __builtin_amdgcn_raw_buffer_store_b64(o[0], k.ws, k.lane * 8u, so + 512u, AUX_SC1);
        __builtin_amdgcn_raw_buffer_store_b128((u32x4){o[1].x, o[1].y, o[2].x, o[2].y}, k.ws, k.l16, so + 1024u, AUX_SC1);
        __builtin_amdgcn_raw_buffer_store_b128((u32x4){o[3].x, o[3].y, o[4].x, o[4].y}, k.ws, k.l16, so + 2048u, AUX_SC1);
    } else {
        __builtin_amdgcn_raw_buffer_store_b128((u32x4){o[0].x, o[0].y, o[1].x, o[1].y}, k.ws, k.l16, so, AUX_SC1);
        __builtin_amdgcn_raw_buffer_store_b128((u32x4){o[2].x, o[2].y, o[3].x, o[3].y}, k.ws, k.l16, so + 1024u, AUX_SC1);
        __builtin_amdgcn_raw_buffer_store_b64(o[4], k.ws, k.lane * 8u, so + 2048u, AUX_SC1);
    }
}

template <int... NB> DI void res_read_(u32x2 (&rr)[5], iseq<NB...>) { ((rr[NB] = a_read2<120 + 2 * NB>()), ...); }
DI void p24_epilogue(const Ctx& k, LAS char* lds, f32x4 (&acc)[5][2], const Res& dst, bool wt, unsigned st_so) {
    LAS char* mine = lds + L_XCH + k.wv * 5120u + k.l16;
#pragma unroll
    for (int nb = 0; nb < 5; ++nb) *(LAS f32x4*)(mine + nb * 1024) = acc[nb][1];
    u32x2 rr[5];
    res_read_(rr, mkseq<5>{});
    f32x4 v[5];
#pragma unroll
    for (int nb = 0; nb < 5; ++nb) {
        v[nb] = acc[nb][0] + (f32x4){bflo(rr[nb].x), bfhi(rr[nb].x), bflo(rr[nb].y), bfhi(rr[nb].y)};
        asm volatile("" : "+v"(v[nb]));
    }
    bar();
    const LAS char* part = lds + L_XCH + (k.wv ^ 2u) * 5120u + k.l16;
    f32x4 o[5];
#pragma unroll
    for (int nb = 0; nb < 5; ++nb) o[nb] = *(const LAS f32x4*)(part + nb * 1024);
    u32x2 pk[5];
#pragma unroll
    for (int nb = 0; nb < 5; ++nb) { const f32x4 t = v[nb] + o[nb]; pk[nb] = (u32x2){cvtpk(t[0], t[1]), cvtpk(t[2], t[3])}; }
    if (wt) {
        const unsigned vo16 = (k.lane >> 5) * 1024u + (((k.lane >> 4) & 1) * 32 + (k.wv >> 1) * 16 + (k.lane & 15)) * 16u;
#pragma unroll
        for (int nb = 0; nb < 4; nb += 2) {
            const auto s0 = __builtin_amdgcn_permlane32_swap(pk[nb].x, pk[nb + 1].x, false, false), s1 = __builtin_amdgcn_permlane32_swap(pk[nb].y, pk[nb + 1].y, false, false);
            __builtin_amdgcn_raw_buffer_store_b128((u32x4){s0[0], s1[0], s0[1], s1[1]}, dst.srd, vo16, dst.so + nb * 1024u, AUX_SC1);
        }
        {
            const auto s0 = __builtin_amdgcn_permlane32_swap(pk[4].x, pk[4].x, false, false), s1 = __builtin_amdgcn_permlane32_swap(pk[4].y, pk[4].y, false, false);
            if (k.lane < 32) __builtin_amdgcn_raw_buffer_store_b128((u32x4){pk[4].x, pk[4].y, s0[1], s1[1]}, dst.srd, vo16, dst.so + 4 * 1024u, AUX_SC1);
        }
        float s = 0.f, q = 0.f;
#pragma unroll
        for (int nb = 0; nb < 5; ++nb) {
            const unsigned x = pk[nb].x, y = pk[nb].y;
            s = dot2(y, 0x3f803f80u, dot2(x, 0x3f803f80u, s));
            q = dot2(y, y, dot2(x, x, q));
        }
        s = xsum32(xsum16(s)); q = xsum32(xsum16(q));
        if (k.lane < 16) __builtin_amdgcn_raw_buffer_store_b64((u32x2){__float_as_uint(s), __float_as_uint(q)}, k.ws, k.lane * 8u, st_so, AUX_SC1);
    } else {
        const unsigned row = k.lane >> 4, vo16 = dst.vo - 8u * row + (row & 1) * 32u + (row >> 1) * 16u;
#pragma unroll
        for (int nb = 0; nb < 4; nb += 2) {
            const auto s0 = __builtin_amdgcn_permlane16_swap(pk[nb].x, pk[nb + 1].x, false, false), s1 = __builtin_amdgcn_permlane16_swap(pk[nb].y, pk[nb + 1].y, false, false);
            __builtin_amdgcn_raw_buffer_store_b128((u32x4){s0[0], s1[0], s0[1], s1[1]}, dst.srd, vo16, dst.so + nb * 32u, 0);
        }
        __builtin_amdgcn_raw_buffer_store_b64(pk[4], dst.srd, dst.vo, dst.so + 4 * 32u, 0);
    }
}

DI void p3_block(const Ctx& k, const LAS char* lds, const f32x16& acc, const Stat st, unsigned bl, unsigned so) {
    const unsigned h = k.lane >> 5;
    const LAS char* cs = lds + L_CSG + bl * 128u + 16u * h;
    const f32x4 g0 = *(const LAS f32x4*)(cs), g1 = *(const LAS f32x4*)(cs + 32), u0 = *(const LAS f32x4*)(cs + 64), u1 = *(const LAS f32x4*)(cs + 96);
    const float r2 = st.rstd * st.rstd;
    float v[8];
#pragma unroll
    for (int i = 0; i < 4; ++i) {
        v[i]     = fmaf(st.nmean, g0[i], acc[i])     * r2 * fmaf(st.nmean, u0[i], acc[8 + i]);
        v[4 + i] = fmaf(st.nmean, g1[i], acc[4 + i]) * r2 * fmaf(st.nmean, u1[i], acc[12 + i]);
    }
    const u32x4 o = {cvtpk(v[0], v[1]), cvtpk(v[2], v[3]), cvtpk(v[4], v[5]), cvtpk(v[6], v[7])};
    __builtin_amdgcn_raw_buffer_store_b128(o, k.ws, k.l16, so, AUX_SC1);
}
struct Hook3 {
    LAS char* lds; Stat st; StatSteps ss; float r2; unsigned hso, fh, vcs, cso, sto; Act16 a;
    template <int I> DI void post_load(const Ctx& k) {
        if constexpr (I < 4) a_load1<244 + I>(k.ws, (k.wv * 64 + k.lane) * 4u, sadd<I * 3456 * 4u>(cso));
        else a_load2<248 + 2 * (I - 4)>(k.ws, (k.lane & 31) * 8u + (k.lane >> 5) * 1024u, sadd<(I - 4) * 256u>(sto));
    }
    u32x4 c[2][4]; float g, u, v[2]; u32x4 o; unsigned fh0;
    using M = Mdl<3>; using S = Seq<3>;
    template <int F> static constexpr int cover() { return (F == 80 + 69 || F == 160 + 69) ? M::before(F) - 1 - M::idx_extra(F - 69 + 44) : 63; }
    template <int F, int F0, int D, int AI, int BL, int CB> DI void block(const Ctx& k, f32x16 (&acc)[4]) {
        if constexpr (F >= F0 && F < F0 + 4) wread<BL * 128 + (F - F0) * 32>(c[CB][F - F0], vcs);
        if constexpr (F >= F0 + D && F < F0 + D + 16) {
            constexpr int e = F - F0 - D, i = e >> 1;
            if constexpr (e == 0) asm volatile("" : "+v"(c[CB][0]), "+v"(c[CB][2]));
            if constexpr (e == 8) asm volatile("" : "+v"(c[CB][1]), "+v"(c[CB][3]));
            if constexpr ((e & 1) == 0) {
                g = fmaf(st.nmean, __uint_as_float(c[CB][i >> 2][i & 3]), acc[AI][i]);
                u = fmaf(st.nmean, __uint_as_float(c[CB][2 + (i >> 2)][i & 3]), acc[AI][8 + i]);
                asm volatile("" : "+v"(g), "+v"(u));
            } else {
                v[i & 1] = g * r2 * u;
                if constexpr (i & 1) { unsigned pk = cvtpk(v[0], v[1]); asm volatile("" : "+v"(pk)); o[i >> 1] = pk; }
                else asm volatile("" : "+v"(v[0]));
            }
        }
        if constexpr (F == F0 + D + 16) __builtin_amdgcn_raw_buffer_store_b128(o, k.ws, k.l16, sadd<BL * 1024u>(hso), AUX_SC1);
    }
    template <int F> DI void run(const Ctx& k, f32x16 (&acc)[4]) {
        if constexpr (F == FS) vmwait<M::before(F) + M::dma(F) + M::act(F) + M::post(F) - 1 - M::idx_post_last()>();
        if constexpr (F >= FS && F < FS + 8) ss.template step<F - FS>(k, st);
        if constexpr (F == FS + 8) { r2 = st.rstd * st.rstd; asm volatile("" : "+v"(r2)); }
        if constexpr (F == FS + 10) { c[0][0] = a_read4<244>();  }
        if constexpr (F == FS + 11) { *(LAS float*)(lds + L_CSG + (k.wv * 64 + k.lane) * 4u) = sum4(c[0][0]); asm volatile("; tblwr 2" ::: "memory"); }
        if constexpr (F == 82) asm volatile("; tblrd 2" ::: "memory");
        if constexpr (F >= 80 && F < 240) {
            constexpr int p = F / 80 - 1, R = F - F % 80;
            block<F, R + 2, 6, 2 * (p & 1), 2 * p, 0>(k, acc);
            block<F, R + 22, 6, 2 * (p & 1) + 1, 2 * p + 1, 1>(k, acc);
            if constexpr (F % 80 == 69) { if (k.wv == 0) post16(k, fh0 + k.j * 8u + p * 256 * 128u, 128u); }
        }
        if constexpr (F >= 240) { block<F, 240, 6, 0, 4, 0>(k, acc); block<F, 256, 6, 1, 5, 1>(k, acc); }
        if constexpr (F == S::FPOLL) a_load2<240>(k.ws, (k.lane & 15) * 8u, fh);
        if constexpr (F == S::FCHK) {
            vmwait<M::before(F) + M::dma(F) + M::act(F) + S::store(F) - 1 - M::idx_extra(S::FPOLL) - S::store(S::FPOLL)>();
            if (!flags_ok<240>(k)) poll(k, fh, 16);
        }
        if constexpr (F == S::FPOLL1) a_load2<238>(k.ws, (k.lane & 15) * 8u, fh + 256 * 128u);
        if constexpr (S::pre16(F)) { constexpr int i = (F - S::FPRE) >> 1; act16_load<true, (i >> 1), (i & 1)>(k, a); }
    }
};

struct Role16 { unsigned tb, cs, kh, t32vo; };
DI Role16 role16(const Ctx& k) {
    Role16 q; const unsigned t16 = k.lane & 15, g = k.lane >> 4;
    q.cs = cs_of(k.j); q.kh = k.wv >> 1; q.tb = 4 * k.c + 2 * ((k.j >> 2) & 1) + (k.wv & 1);
    q.t32vo = ((g & 1) * 32 + 16 * q.kh + t16) * 16u + (g >> 1) * 8u;
    return q;
}
DI Act16 act16_addr(const Ctx& k, const Role16& q) {
    Act16 a; const unsigned t16 = k.lane & 15, g = k.lane >> 4, base = (g >> 1) * 1024u + (g & 1) * 512u + t16 * 16u;
    a.vp[0] = base + q.kh * 256u; a.vp[1] = base + (q.kh ^ 1) * 256u;
    a.vs[0] = a.vp[0] + (g >> 1) * 6144u; a.vs[1] = a.vp[1] + (g >> 1) * 6144u; a.so = 0;
#pragma unroll
    for (int w = 0; w < 2; ++w)
#pragma unroll
        for (int t = 0; t < 2; ++t) {
            const bool sh = (g >> 1) == (unsigned)w;
            const unsigned alt = (g >> 1) * 1024u + ((g & 1) * 32u + 16u * (q.kh ^ t) + t16) * 8u;
            a.vq[w][t][0] = sh ? alt : a.vp[t]; a.vq[w][t][1] = sh ? alt + 512u : a.vp[t] + 8u;
        }
    return a;
}

DI void touch_flags(const Ctx& k) {
    const unsigned i = k.lane, own = 16 * k.c + k.j;
    const unsigned line = i < 12 ? F_XI + 256 * i + own : i < 16 ? F_KV + (((i - 12) >> 1) * 256 + 16 * k.j + k.c) * 2 + (i & 1) : i == 16 ? F_TQ + 16 * k.j + k.c : F_TO + k.j;
    if (i < 18) asm volatile("buffer_load_dword a254, %0, %1, 0 offen sc1" :: "v"(W_FLAG + line * 128u), "s"(k.ws) : "memory", ACLOB);
}

__global__ __launch_bounds__(256, 1) void fused_fwd(Ptrs p) {
    extern __shared__ __attribute__((aligned(16))) char lds_raw[];
    LAS char* lds = (LAS char*)lds_raw;
    const unsigned b = blockIdx.x;
    Ctx k;
    k.lane = threadIdx.x & 63; k.l16 = k.lane * 16; k.wv = rfl(threadIdx.x >> 6);
    k.ws = mksrd(p.ws, W_END);
    { const unsigned x = b & 7, i = b >> 3; k.c = 4 * (x >> 1) + (i >> 3); k.j = 8 * (x & 1) + (i & 7); }

    if (k.wv < 2) joba<0>(k, p, lds); else if (k.wv == 3) touch_flags(k);
    {
        const u32x2 e = __builtin_amdgcn_raw_buffer_load_b64(k.ws, 0, W_EPOCH, AUX_SC1);
        const unsigned long long t = (((unsigned long long)rfl(e.y) << 32) | rfl(e.x)) + 1ull;
        k.tlo = (unsigned)t; k.thi = (unsigned)(t >> 32);
    }
    for (unsigned i = threadIdx.x; i < 6656 / 16; i += 256) *(LAS u32x4*)(lds + L_AM + i * 16) = (u32x4){0u, 0u, 0u, 0u};
    vmwait<0>();
    bar();

    if (k.wv < 2) joba<1>(k, p, lds);
    rawbar();
    if (k.wv >= 2) jobb_issue(k, p, b); else joba_post(k);

    vmwait<0>();
    bar();

    jobb_emit(k, lds, b);
    if (b >= 162 && b < 194) {
        const unsigned idx = b - 162, cs = idx >> 2, j0 = 8 * (cs >> 2) + (cs & 3);
        vmwait<0>();
        bar();
        if (k.wv == 0) { post(k, FL(F_TO + j0) + (idx & 3) * 8u); post(k, FL(F_TO + j0 + 4) + (idx & 3) * 8u); }
    }

    if (k.wv == 0) poll_at(k, k.lane < 16 ? FL(F_TQ + k.j * 16 + k.c) + k.lane * 8u : FL(F_XI + k.c * 16 + k.j) + (k.lane & 15) * 8u);
    bar();

    {
        const Dma d = mkdma(k); const unsigned wq = seg_wq(k);
        dma_chunk<slot_addr(SB1, 0)>(k, d, wq); dma_chunk<slot_addr(SB1, 1)>(k, d, wq + CH); dma_chunk<slot_addr(SB1, 2)>(k, d, wq + 2 * CH);
        dma_chunk<slot_addr(SB1, 3)>(k, d, wq + 3 * CH); dma_chunk<slot_addr(SB1, 4)>(k, d, wq + 4 * CH); dma_chunk<slot_addr(SB1, 5)>(k, d, wq + 5 * CH);
    }

#pragma unroll 1
    for (unsigned layer = 0; layer < 2; ++layer) {
        u32x2 pq[5];

        {
            fresh(k);

            const unsigned tbw = 4 * k.c + k.wv;
            const unsigned aso = opq(W_XI + layer * XL + tbw * XT);
            act32_init(k, aso, mkseq<NI>{});
            const Dma d = mkdma(k); Segs sg; sg.cur = opq(seg_wq(k)); sg.nxt = opq(seg_wo(k)); sg.on = true; sg.so = 0;
            Rd s; f32x16 acc[4]; Hook1 hk;
            hk.lds = lds; hk.vcs = L_CSQ + (k.lane >> 5) * 16u; hk.row = lds + L_KV + (32 * k.wv + (k.lane & 31)) * 176u + 8u * (k.lane >> 5);
            hk.fto = opq(FL(F_TO + k.j)); hk.cso = opq(W_CSQ + k.j * 512u); hk.sto = opq(W_STI + layer * STL + tbw * 2048u);
            start32<1>(k, s);
            run32<1, 0, 160>(k, d, sg, s, acc, aso, hk);
            bar();

            fresh(k);
            if (k.wv < 2) {
                kv_partial(k, layer);
                vmwait<0>();
                post16<false>(k, FL(F_KV + (layer * 16 + k.j) * 32) + (k.c * 2 + k.wv) * 8u, 256u);

            }
            else {
                const unsigned src = seg_wo(k) + 4 * CH;
#pragma unroll 1
                for (unsigned i = k.wv - 2; i < 20; i += 2) {
                    const unsigned m0v = slot_addr(SB2, 4) + i * 1024u, so = src + i * 1024u;
                    asm volatile("s_mov_b32 m0, %0\n\ts_nop 0\n\tbuffer_load_dwordx4 %1, %2, %3 offen sc1 lds ; ringwr %c4" :: "s"(m0v), "v"(k.l16), "s"(k.ws), "s"(so), "i"(slot_addr(SB2, 4) / CH) : "memory");
                }
            }
            mfma_done(); pin(acc[2]); pin(acc[3]);
            q_epilogue(k, lds, acc, hk.st, pq);

        }

        if (k.wv == 0) poll(k, FL(F_KV + ((layer * 16 + k.j) * 16 + k.c) * 2), 32);
        vmwait<0>();
        bar();

        fresh(k);
        p1b(k, lds, layer, pq);

        fresh(k);
        publish(k, FL(F_O + (layer * 16 + k.c) * 16));

        {
            fresh(k);
            const Role16 q = role16(k);
            Act16 a = act16_addr(k, q);
            Res src; src.srd = k.ws; src.vo = q.t32vo; src.so = opq(W_XI + layer * XL + q.tb * XT + 5 * q.cs * 1024u); src.stride = 1024;
            a.so = opq(W_O + layer * XL + q.tb * XT + q.kh * 20480u);
            asm volatile("" : "+v"(a.vp[0]), "+v"(a.vp[1]), "+v"(src.vo));
            asm volatile("" : "+v"(a.vq[0][0][0]), "+v"(a.vq[0][0][1]), "+v"(a.vq[0][1][0]), "+v"(a.vq[0][1][1]), "+v"(a.vq[1][0][0]), "+v"(a.vq[1][0][1]), "+v"(a.vq[1][1][0]), "+v"(a.vq[1][1][1]));
            if (k.wv == 0) poll(k, FL(F_O + (layer * 16 + k.c) * 16 + k.j), 16);
            bar();

            const Dma d = mkdma(k); Segs sg; sg.cur = 0; sg.nxt = opq(seg_wgu(k)); sg.on = true; sg.so = 0;
            f32x4 acc[5][2];
            pass16<false>(k, d, sg, a, src, 0, acc);

            Res dst; dst.srd = k.ws; dst.vo = q.t32vo; dst.so = W_X1 + layer * XL + q.tb * XT + 5 * q.cs * 1024u; dst.stride = 1024;
            p24_epilogue(k, lds, acc, dst, true, W_ST1 + layer * STL + q.tb * 2048u + q.cs * 256u + q.kh * 128u);

        }
        fresh(k);
        publish(k, FL(F_X1 + (layer * 16 + k.c) * 16));

        { const unsigned wgu = seg_wgu(k);
          dma_bulk<slot_addr(SB3, 2)>(k, wgu + 2 * CH); dma_bulk<slot_addr(SB3, 3)>(k, wgu + 3 * CH); dma_bulk<slot_addr(SB3, 4)>(k, wgu + 4 * CH); }
        {
            fresh(k);
            const unsigned tbw = 4 * k.c + k.wv, m = k.j & 7, B0 = 54 * (k.j >> 3) + (m <= 6 ? 7 * m : 48u);
            const bool seven = m < 6;
            const unsigned aso = opq(W_X1 + layer * XL + tbw * XT);
            if (k.wv == 0) poll(k, FL(F_X1 + (layer * 16 + k.c) * 16 + k.j), 16);
            bar();

            act32_init(k, aso, mkseq<NI>{});
            const Dma d = mkdma(k); Segs sg; sg.cur = opq(seg_wgu(k)); sg.nxt = opq(seg_wd(k)); sg.on = true; sg.so = 0;
            Rd s; f32x16 acc[4]; Hook3 hk;
            hk.lds = lds; hk.hso = opq(W_H + layer * HL + tbw * HT + B0 * 1024u); hk.fh0 = opq(FL(F_H + (layer * 48 + k.c) * 16)); hk.fh = opq(FL(F_H + (layer * 48 + k.c) * 16 + k.j)); hk.vcs = L_CSG + (k.lane >> 5) * 16u;
            hk.cso = opq(W_CSG + B0 * 128u); hk.sto = opq(W_ST1 + layer * STL + tbw * 2048u);
            const Role16 q = role16(k);
            hk.a = act16_addr(k, q); hk.a.so = opq(W_H + layer * HL + q.tb * HT + q.kh * 54 * 1024u);
            Res src; src.srd = k.ws; src.vo = q.t32vo; src.so = opq(W_X1 + layer * XL + q.tb * XT + 5 * q.cs * 1024u); src.stride = 1024;
            const unsigned wq_next = opq(seg_wq(k));
            start32<3>(k, s);
            run32<3, 0, 240>(k, d, sg, s, acc, aso, hk);
            const unsigned wd = sg.nxt;
            if (seven) {
                asm volatile("; excl a");
                run32<3, 240, 280>(k, d, sg, s, acc, aso, hk);
                mfma_done(); pin(acc[2]);
                p3_block(k, lds, acc[2], hk.st, 6, hk.hso + 6 * 1024u);
                vmwait<8>();
                bar();
            } else {
                asm volatile("; excl b");
                asm volatile("s_waitcnt lgkmcnt(0)" : "+v"(s.w[0]), "+v"(s.w[1]), "+v"(s.w[2]), "+v"(s.w[3]), "+v"(s.w[4]) :: "memory");
                mfma_done(); pin(acc[0]); pin(acc[1]);
                p3_block(k, lds, acc[0], hk.st, 4, hk.hso + 4 * 1024u); p3_block(k, lds, acc[1], hk.st, 5, hk.hso + 5 * 1024u);
                a_load2<238>(k.ws, (k.lane & 15) * 8u, hk.fh + 256 * 128u);
                vmwait<3>();
                bar();
                dma_chunk<slot_addr(SB4, 3)>(k, d, wd + 3 * CH); dma_chunk<slot_addr(SB4, 4)>(k, d, wd + 4 * CH);
            }
            asm volatile("; excl end");

            dma_chunk<slot_addr(SB4, 5)>(k, d, wd + 5 * CH);


            const Act16& a = hk.a;
            Segs sg4; sg4.cur = wd; sg4.nxt = wq_next; sg4.on = layer == 0; sg4.so = 0;
            f32x4 acc4[5][2];
            pass16<true>(k, d, sg4, a, src, hk.fh, acc4);

            fresh(k);
            Res dst;
            if (layer == 0) { dst.srd = k.ws; dst.vo = q.t32vo; dst.so = W_XI + XL + q.tb * XT + 5 * q.cs * 1024u; dst.stride = 1024; }
            else { dst.srd = mksrd(p.out, 2048 * 640 * 2); dst.vo = (32 * q.tb + 16 * q.kh + (k.lane & 15)) * 1280u + (80 * q.cs + 4 * (k.lane >> 4)) * 2u; dst.so = 0; dst.stride = 32; }
            p24_epilogue(k, lds, acc4, dst, layer == 0, W_STI + STL + q.tb * 2048u + q.cs * 256u + q.kh * 128u);
        }

        if (layer == 0) {
            fresh(k);
            publish(k, FL(F_XI + (16 + k.c) * 16));
            { const unsigned wq = seg_wq(k); dma_bulk<slot_addr(SB1, 3)>(k, wq + 3 * CH); dma_bulk<slot_addr(SB1, 4)>(k, wq + 4 * CH); }
            if (k.wv == 0) poll(k, FL(F_XI + (16 + k.c) * 16 + k.j), 16);
            bar();
        }

    }
    if (b == 0 && k.wv == 0 && k.lane == 0) __builtin_amdgcn_raw_buffer_store_b64((u32x2){k.tlo, k.thi}, k.ws, W_EPOCH, 0, AUX_SC1);
}

}

extern "C" void kernel_launch(void* const* d_in, const int* in_sizes, int n_in, void* d_out, int out_size, void* d_ws, size_t ws_size, hipStream_t stream) {
    mk::Ptrs p;
    p.x0 = d_in[0]; p.wqkv = d_in[1]; p.wo = d_in[2]; p.wg = d_in[4]; p.wu = d_in[5]; p.wd = d_in[6]; p.out = d_out; p.ws = d_ws;
    (void)hipFuncSetAttribute((const void*)mk::fused_fwd, hipFuncAttributeMaxDynamicSharedMemorySize, mk::LDS_BYTES);
    mk::fused_fwd<<<256, 256, mk::LDS_BYTES, stream>>>(p);
}
```
